```python
import torch

def _softmax(x, axis=-1):
    return torch.softmax(x, dim=axis)

def _gelu(x):
    return torch.nn.functional.gelu(x, approximate='tanh')

def _silu(x):
    return torch.nn.functional.silu(x)

def _sigmoid(x):
    return torch.sigmoid(x)

def _layernorm(x, axis=-1, eps=1e-05):
    m = x.mean(dim=axis, keepdim=True)
    v = x.var(dim=axis, keepdim=True, unbiased=False)
    return (x - m) / torch.sqrt(v + eps)

def _scatter_add(tgt, idx, src, axis=0):
    return tgt.clone().index_add_(axis, idx, src)

def _segment_sum(src, seg, n):
    out = torch.zeros((n,) + src.shape[1:], dtype=src.dtype)
    return out.index_add_(0, seg, src)

def _pad(x, pw):
    flat = tuple((v for pair in reversed(pw) for v in pair))
    return torch.nn.functional.pad(x, flat)
kernel_name = 'standalone__window_attn_twins__vision'

def setup_inputs():

    _w = lambda a: (lambda t: t.to(torch.bfloat16) if t.is_floating_point() else t)(torch.as_tensor(a))
    H = W = 224 // 8 * 8
    C = 96
    return {'x': _w((lambda *s: torch.randn(*s, dtype=torch.bfloat16) * 0.8)(1, H, W, C)), 'wq': _w((lambda *s: torch.randn(*s, dtype=torch.bfloat16) * 0.8)(C, C)), 'wkv': _w((lambda *s: torch.randn(*s, dtype=torch.bfloat16) * 0.8)(C, 2 * C)), 'wo': _w((lambda *s: torch.randn(*s, dtype=torch.bfloat16) * 0.05)(C, C))}

def reference(x, wq, wkv, wo):
    B, H, W, C = x.shape
    win = 8
    nh, nw = (H // win, W // win)
    xs = x.reshape(B, nh, win, nw, win, C).mean(axis=(2, 4)).reshape(B, nh * nw, C)
    q = x.reshape(B, H * W, C) @ wq
    kv = xs @ wkv
    k, v = (kv[..., :C].clone(), kv[..., C:].clone())
    scale = C ** (-0.5)
    p = _softmax(torch.einsum('bqc,bkc->bqk', q, k) * scale, axis=-1)
    o = torch.einsum('bqk,bkc->bqc', p, v) @ wo
    return o.reshape(B, H, W, C)
```

```cpp
#include <hip/hip_runtime.h>
#include <hip/hip_bf16.h>
typedef float    f32x4  __attribute__((ext_vector_type(4)));
typedef float    f32x2  __attribute__((ext_vector_type(2)));
typedef __bf16   bf16x8 __attribute__((ext_vector_type(8)));
typedef __bf16   bf16x2 __attribute__((ext_vector_type(2)));
typedef unsigned u32x4  __attribute__((ext_vector_type(4)));
typedef unsigned u32x2 __attribute__((ext_vector_type(2)));
typedef int   i32x4  __attribute__((ext_vector_type(4)));
#define RFL(v) __builtin_amdgcn_readfirstlane(v)
#define DI __device__ __forceinline__
#define NREP 8
#define FSTR 256
constexpr int NBLK_MAIN = 196, IMG = 224, TILEB = 12288, NTILE = 25;
constexpr unsigned FLAGS = 0x80000, EPOCH = 0x7f000, XS = 135168, XT = XS + 1024, LDSB = XT + 1024;

DI unsigned pk2(float lo, float hi) { f32x2 v = {lo, hi}; bf16x2 b = __builtin_convertvector(v, bf16x2); return __builtin_bit_cast(unsigned, b); }
DI float bflo(unsigned u) { return __builtin_bit_cast(float, u << 16); }
DI float bfhi(unsigned u) { return __builtin_bit_cast(float, u & 0xffff0000u); }
DI f32x4 mfma16(u32x4 a, u32x4 b, f32x4 c) {
    return __builtin_amdgcn_mfma_f32_16x16x32_bf16(__builtin_bit_cast(bf16x8, a), __builtin_bit_cast(bf16x8, b), c, 0, 0, 0); }

DI void slow_tile(const char* pk, int q, const char* ws, char* __restrict__ op, int lane) {
    const int l15 = lane & 15, g = lane >> 4;
    const int kpos = ((l15 >> 2) * 16 + g * 4 + (l15 & 3)) * 16;
    u32x4 qf[3];
#pragma unroll
    for (int u = 0; u < 3; ++u) qf[u] = *(const u32x4*)(pk + (q * 3 + u) * 1024);
    float mx = -INFINITY;
    for (int h = 0; h < 49; ++h) {
        const char* kb = ws + (size_t)(h >> 1) * TILEB + (h & 1) * 3072 + kpos;
        f32x4 s = {0, 0, 0, 0};
#pragma unroll
        for (int u = 0; u < 3; ++u) s = mfma16(*(const u32x4*)(kb + u * 1024), qf[u], s);
        mx = fmaxf(fmaxf(mx, fmaxf(s[0], s[1])), fmaxf(s[2], s[3]));
    }
    mx = fmaxf(mx, __shfl_xor(mx, 16)); mx = fmaxf(mx, __shfl_xor(mx, 32));
    f32x4 o[6]; float l = 0.f;
#pragma unroll
    for (int d = 0; d < 6; ++d) o[d] = (f32x4){0, 0, 0, 0};
    for (int t = 0; t < NTILE; ++t) {
        const char* kb = ws + (size_t)t * TILEB + kpos;
        const char* vb = ws + (size_t)t * TILEB + 6144 + lane * 8;
        f32x4 s0 = {-mx, -mx, -mx, -mx}, s1 = s0;
#pragma unroll
        for (int u = 0; u < 3; ++u) { s0 = mfma16(*(const u32x4*)(kb + u * 1024), qf[u], s0); s1 = mfma16(*(const u32x4*)(kb + 3072 + u * 1024), qf[u], s1); }
        u32x4 p;
        p[0] = pk2(__builtin_amdgcn_exp2f(s0[0]), __builtin_amdgcn_exp2f(s0[1])); p[1] = pk2(__builtin_amdgcn_exp2f(s0[2]), __builtin_amdgcn_exp2f(s0[3]));
        p[2] = pk2(__builtin_amdgcn_exp2f(s1[0]), __builtin_amdgcn_exp2f(s1[1])); p[3] = pk2(__builtin_amdgcn_exp2f(s1[2]), __builtin_amdgcn_exp2f(s1[3]));
        if (t == NTILE - 1) { p[2] = 0; p[3] = 0; }
#pragma unroll
        for (int j = 0; j < 4; ++j) l += bflo(p[j]) + bfhi(p[j]);
#pragma unroll
        for (int d = 0; d < 6; ++d) { const u32x2 a = *(const u32x2*)(vb + d * 1024), b = *(const u32x2*)(vb + d * 1024 + 512);
            o[d] = mfma16((u32x4){a[0], a[1], b[0], b[1]}, p, o[d]); }
    }
    l += __shfl_xor(l, 16); l += __shfl_xor(l, 32);
    const float inv = 1.0f / l;
    u32x4 ob[3];
#pragma unroll
    for (int u = 0; u < 3; ++u) ob[u] = (u32x4){pk2(o[2 * u][0], o[2 * u][1]), pk2(o[2 * u][2], o[2 * u][3]), pk2(o[2 * u + 1][0], o[2 * u + 1][1]), pk2(o[2 * u + 1][2], o[2 * u + 1][3])};
#pragma unroll
    for (int p = 0; p < 3; ++p) { f32x4 a = {0, 0, 0, 0}, b = a;
#pragma unroll
        for (int u = 0; u < 3; ++u) { a = mfma16(*(const u32x4*)(pk + (12 + 6 * p + u) * 1024), ob[u], a); b = mfma16(*(const u32x4*)(pk + (15 + 6 * p + u) * 1024), ob[u], b); }
        a *= inv; b *= inv;
        *(u32x4*)(op + p * 64) = (u32x4){pk2(a[0], a[1]), pk2(a[2], a[3]), pk2(b[0], b[1]), pk2(b[2], b[3])}; }
}

__global__ __launch_bounds__(256, 1) void attn_kernel(const char* __restrict__ x, const char* __restrict__ wq, const char* __restrict__ wkv,
                                                      const char* __restrict__ wo, char* ws, char* __restrict__ out) {
    extern __shared__ __attribute__((aligned(16))) char lds[];
    const int tid = threadIdx.x, wid = __builtin_amdgcn_readfirstlane(tid >> 6), blk = blockIdx.x == 0 ? NBLK_MAIN : blockIdx.x - 1;
    int lane = tid & 63;
    asm volatile("s_getpc_b64 s[48:49]\n\tv_lshlrev_b32 v224, 6, %0\n\tglobal_load_dword v224, v224, s[48:49]" :: "v"(tid) : "s48", "s49", "v224");
    unsigned qa, qa2, lane16 = lane * 16;
    {   const int wr = blk / 7, wc = (blk - wr * 7) * 4 + wid;
        qa = (unsigned)((wr * 8 + ((lane >> 3) & 1)) * IMG + wc * 8 + (lane & 7)) * 192u + (lane >> 4) * 16u; qa2 = qa;
        asm volatile("" : "+{v5}"(qa2), "+{v7}"(lane16));
    }
    __builtin_amdgcn_sched_barrier(0);
    asm volatile("" :: "s"(x), "s"(wq), "s"(wkv), "s"(wo), "s"(ws), "s"(out));

    const unsigned long long wsa = (unsigned long long)ws;
    const i32x4 srd = {(int)(unsigned)wsa, (int)((unsigned)(wsa >> 32) & 0xffffu), (NTILE + 2) * TILEB, 0x00020000};
    if (blk == NBLK_MAIN) {
        const int l15 = lane & 15, g = lane >> 4;
        const unsigned vkv = (32 * (wid - 1) + 8 * (l15 >> 2) + (l15 & 3)) * 384 + g * 16, vo = (32 * (wid - 1) + 8 * g + (l15 >> 2)) * 192 + (l15 & 3) * 16;
        const unsigned pn = NTILE * TILEB + (wid - 1) * 6144 + 3072 + (wid == 2 ? lane : (l15 >> 2) * 16 + g * 4 + (l15 & 3)) * 16;
        asm volatile(
        "s_nop 4\n\t"
    "s_cmp_eq_u32 s36, 0\n\t"
    "s_cbranch_scc1 2f\n\t"
    "s_add_u32 m0, s34, 98112\n\t"
    "s_nop 0\n\t"
    "global_load_lds_dwordx4 v209, s[30:31] offset:192\n\t"
    "s_add_u32 m0, s34, 99072\n\t"
    "s_nop 0\n\t"
    "global_load_lds_dwordx4 v209, s[30:31] offset:256\n\t"
    "s_add_u32 m0, s34, 100032\n\t"
    "s_nop 0\n\t"
    "global_load_lds_dwordx4 v209, s[30:31] offset:320\n\t"
    "s_add_u32 m0, s34, 99648\n\t"
    "s_nop 0\n\t"
    "global_load_lds_dwordx4 v209, s[30:31] offset:1728\n\t"
    "s_add_u32 m0, s34, 100608\n\t"
    "s_nop 0\n\t"
    "global_load_lds_dwordx4 v209, s[30:31] offset:1792\n\t"
    "s_add_u32 m0, s34, 101568\n\t"
    "s_nop 0\n\t"
    "global_load_lds_dwordx4 v209, s[30:31] offset:1856\n\t"
    "s_add_u32 m0, s34, 116736\n\t"
    "s_nop 0\n\t"
    "global_load_lds_dwordx4 v210, s[32:33] offset:0\n\t"
    "s_add_u32 m0, s34, 117696\n\t"
    "s_nop 0\n\t"
    "global_load_lds_dwordx4 v210, s[32:33] offset:64\n\t"
    "s_add_u32 m0, s34, 118656\n\t"
    "s_nop 0\n\t"
    "global_load_lds_dwordx4 v210, s[32:33] offset:128\n\t"
    "s_add_u32 m0, s34, 119040\n\t"
    "s_nop 0\n\t"
    "global_load_lds_dwordx4 v210, s[32:33] offset:768\n\t"
    "s_add_u32 m0, s34, 120000\n\t"
    "s_nop 0\n\t"
    "global_load_lds_dwordx4 v210, s[32:33] offset:832\n\t"
    "s_add_u32 m0, s34, 120960\n\t"
    "s_nop 0\n\t"
    "global_load_lds_dwordx4 v210, s[32:33] offset:896\n\t"
    "s_waitcnt vmcnt(0)\n\t"
    "s_barrier\n\t"
    "v_add_u32 v225, 98304, v7\n\t"
    "v_lshl_add_u32 v222, s36, 10, v225\n\t"
    "v_add_u32 v224, 98304, v220\n\t"
    "ds_read_b128 a[172:175], v225 offset:0\n\t"
    "ds_read_b64_tr_b16 v[48:49], v222 offset:17408\n\t"
    "ds_read_b64_tr_b16 v[50:51], v222 offset:20480\n\t"
    "ds_read_b64_tr_b16 v[60:61], v222 offset:17416\n\t"
    "ds_read_b64_tr_b16 v[62:63], v222 offset:20488\n\t"
    "ds_read_b128 a[184:187], v225 offset:3072\n\t"
    "ds_read_b128 a[176:179], v225 offset:1024\n\t"
    "ds_read_b64_tr_b16 v[52:53], v222 offset:23552\n\t"
    "ds_read_b64_tr_b16 v[54:55], v222 offset:26624\n\t"
    "ds_read_b64_tr_b16 v[64:65], v222 offset:23560\n\t"
    "ds_read_b64_tr_b16 v[66:67], v222 offset:26632\n\t"
    "ds_read_b128 a[188:191], v225 offset:4096\n\t"
    "ds_read_b128 a[180:183], v225 offset:2048\n\t"
    "ds_read_b64_tr_b16 v[56:57], v222 offset:29696\n\t"
    "ds_read_b64_tr_b16 v[58:59], v222 offset:32768\n\t"
    "ds_read_b64_tr_b16 v[68:69], v222 offset:29704\n\t"
    "ds_read_b64_tr_b16 v[70:71], v222 offset:32776\n\t"
    "ds_read_b128 a[192:195], v225 offset:5120\n\t"
    "s_waitcnt lgkmcnt(13)\n\t"
    "v_mfma_f32_16x16x32_bf16 v[72:75], a[172:175], v[48:51], 0\n\t"
    "ds_read_b128 a[196:199], v225 offset:6144\n\t"
    "ds_read_b128 a[208:211], v225 offset:9216\n\t"
    "v_mfma_f32_16x16x32_bf16 v[96:99], a[172:175], v[60:63], 0\n\t"
    "s_waitcnt lgkmcnt(14)\n\t"
    "ds_read_b128 a[200:203], v225 offset:7168\n\t"
    "ds_read_b128 a[212:215], v225 offset:10240\n\t"
    "v_mfma_f32_16x16x32_bf16 v[76:79], a[184:187], v[48:51], 0\n\t"
    "ds_read_b128 a[204:207], v225 offset:8192\n\t"
    "ds_read_b128 a[216:219], v225 offset:11264\n\t"
    "v_mfma_f32_16x16x32_bf16 v[100:103], a[184:187], v[60:63], 0\n\t"
    "s_waitcnt lgkmcnt(12)\n\t"
    "ds_read_b128 a[220:223], v225 offset:12288\n\t"
    "ds_read_b128 a[232:235], v225 offset:15360\n\t"
    "v_mfma_f32_16x16x32_bf16 v[72:75], a[176:179], v[52:55], v[72:75]\n\t"
    "ds_read_b128 a[224:227], v225 offset:13312\n\t"
    "ds_read_b128 a[236:239], v225 offset:16384\n\t"
    "v_mfma_f32_16x16x32_bf16 v[96:99], a[176:179], v[64:67], v[96:99]\n\t"
    "ds_read_b128 a[228:231], v225 offset:14336\n\t"
    "ds_read_b128 a[240:243], v225 offset:17408\n\t"
    "v_mfma_f32_16x16x32_bf16 v[76:79], a[188:191], v[52:55], v[76:79]\n\t"
    "v_mfma_f32_16x16x32_bf16 v[100:103], a[188:191], v[64:67], v[100:103]\n\t"
    "s_waitcnt lgkmcnt(12)\n\t"
    "v_mfma_f32_16x16x32_bf16 v[72:75], a[180:183], v[56:59], v[72:75]\n\t"
    "v_mfma_f32_16x16x32_bf16 v[96:99], a[180:183], v[68:71], v[96:99]\n\t"
    "v_mfma_f32_16x16x32_bf16 v[76:79], a[192:195], v[56:59], v[76:79]\n\t"
    "v_mfma_f32_16x16x32_bf16 v[100:103], a[192:195], v[68:71], v[100:103]\n\t"
    "s_waitcnt lgkmcnt(10)\n\t"
    "v_mfma_f32_16x16x32_bf16 v[80:83], a[196:199], v[48:51], 0\n\t"
    "s_nop 2\n\t"
    "v_cvt_pk_bf16_f32 v72, v72, v73\n\t"
    "v_cvt_pk_bf16_f32 v73, v74, v75\n\t"
    "v_mfma_f32_16x16x32_bf16 v[104:107], a[196:199], v[60:63], 0\n\t"
    "v_cvt_pk_bf16_f32 v74, v76, v77\n\t"
    "v_cvt_pk_bf16_f32 v75, v78, v79\n\t"
    "v_mfma_f32_16x16x32_bf16 v[84:87], a[208:211], v[48:51], 0\n\t"
    "v_cvt_pk_bf16_f32 v96, v96, v97\n\t"
    "v_cvt_pk_bf16_f32 v97, v98, v99\n\t"
    "v_mfma_f32_16x16x32_bf16 v[108:111], a[208:211], v[60:63], 0\n\t"
    "s_waitcnt lgkmcnt(8)\n\t"
    "v_cvt_pk_bf16_f32 v98, v100, v101\n\t"
    "v_cvt_pk_bf16_f32 v99, v102, v103\n\t"
    "v_mfma_f32_16x16x32_bf16 v[80:83], a[200:203], v[52:55], v[80:83]\n\t"
    "global_store_dwordx4 v217, v[72:75], s[16:17] offset:-3072 sc1\n\t"
    "global_store_dwordx4 v217, v[96:99], s[16:17] offset:0 sc1\n\t"
    "v_mfma_f32_16x16x32_bf16 v[104:107], a[200:203], v[64:67], v[104:107]\n\t"
    "v_mfma_f32_16x16x32_bf16 v[84:87], a[212:215], v[52:55], v[84:87]\n\t"
    "v_mfma_f32_16x16x32_bf16 v[108:111], a[212:215], v[64:67], v[108:111]\n\t"
    "s_waitcnt lgkmcnt(6)\n\t"
    "v_mfma_f32_16x16x32_bf16 v[80:83], a[204:207], v[56:59], v[80:83]\n\t"
    "v_mfma_f32_16x16x32_bf16 v[104:107], a[204:207], v[68:71], v[104:107]\n\t"
    "v_mfma_f32_16x16x32_bf16 v[84:87], a[216:219], v[56:59], v[84:87]\n\t"
    "v_mfma_f32_16x16x32_bf16 v[108:111], a[216:219], v[68:71], v[108:111]\n\t"
    "s_waitcnt lgkmcnt(4)\n\t"
    "v_mfma_f32_16x16x32_bf16 v[88:91], a[220:223], v[48:51], 0\n\t"
    "s_nop 2\n\t"
    "v_cvt_pk_bf16_f32 v80, v80, v81\n\t"
    "v_cvt_pk_bf16_f32 v81, v82, v83\n\t"
    "v_mfma_f32_16x16x32_bf16 v[112:115], a[220:223], v[60:63], 0\n\t"
    "v_cvt_pk_bf16_f32 v82, v84, v85\n\t"
    "v_cvt_pk_bf16_f32 v83, v86, v87\n\t"
    "v_mfma_f32_16x16x32_bf16 v[92:95], a[232:235], v[48:51], 0\n\t"
    "v_cvt_pk_bf16_f32 v104, v104, v105\n\t"
    "v_cvt_pk_bf16_f32 v105, v106, v107\n\t"
    "v_mfma_f32_16x16x32_bf16 v[116:119], a[232:235], v[60:63], 0\n\t"
    "s_waitcnt lgkmcnt(2)\n\t"
    "v_cvt_pk_bf16_f32 v106, v108, v109\n\t"
    "v_cvt_pk_bf16_f32 v107, v110, v111\n\t"
    "v_mfma_f32_16x16x32_bf16 v[88:91], a[224:227], v[52:55], v[88:91]\n\t"
    "global_store_dwordx4 v217, v[80:83], s[16:17] offset:-2048 sc1\n\t"
    "global_store_dwordx4 v217, v[104:107], s[16:17] offset:1024 sc1\n\t"
    "v_mfma_f32_16x16x32_bf16 v[112:115], a[224:227], v[64:67], v[112:115]\n\t"
    "v_mfma_f32_16x16x32_bf16 v[92:95], a[236:239], v[52:55], v[92:95]\n\t"
    "v_mfma_f32_16x16x32_bf16 v[116:119], a[236:239], v[64:67], v[116:119]\n\t"
    "s_waitcnt lgkmcnt(0)\n\t"
    "v_mfma_f32_16x16x32_bf16 v[88:91], a[228:231], v[56:59], v[88:91]\n\t"
    "v_mfma_f32_16x16x32_bf16 v[112:115], a[228:231], v[68:71], v[112:115]\n\t"
    "v_mfma_f32_16x16x32_bf16 v[92:95], a[240:243], v[56:59], v[92:95]\n\t"
    "v_mfma_f32_16x16x32_bf16 v[116:119], a[240:243], v[68:71], v[116:119]\n\t"
    "s_nop 4\n\t"
    "v_cvt_pk_bf16_f32 v88, v88, v89\n\t"
    "v_cvt_pk_bf16_f32 v89, v90, v91\n\t"
    "v_cvt_pk_bf16_f32 v90, v92, v93\n\t"
    "v_cvt_pk_bf16_f32 v91, v94, v95\n\t"
    "v_cvt_pk_bf16_f32 v112, v112, v113\n\t"
    "v_cvt_pk_bf16_f32 v113, v114, v115\n\t"
    "v_cvt_pk_bf16_f32 v114, v116, v117\n\t"
    "v_cvt_pk_bf16_f32 v115, v118, v119\n\t"
    "global_store_dwordx4 v217, v[88:91], s[16:17] offset:-1024 sc1\n\t"
    "global_store_dwordx4 v217, v[112:115], s[16:17] offset:2048 sc1\n\t"
    "s_waitcnt vmcnt(0)\n\t"
    "s_barrier\n\t"
    "s_branch 3f\n\t"
    "2:\n\t"
    "v_mov_b32 v222, 0x7f000\n\t"
    "global_load_dword v8, v222, s[16:17] sc1\n\t"
    "v_lshl_add_u32 v222, v7, 9, s37\n\t"
    "s_barrier\n\t"
    "s_waitcnt vmcnt(0)\n\t"
    "v_add_u32 v8, 1, v8\n\t"
    "v_mov_b32 v9, v8\n\t"
    "v_mov_b32 v10, v8\n\t"
    "v_mov_b32 v11, v8\n\t"
    "s_barrier\n\t"
    "s_mov_b64 exec, 255\n\t"
    "global_store_dwordx4 v222, v[8:11], s[16:17] sc1\n\t"
    "s_mov_b64 exec, -1\n\t"
    "3:\n\t"
    "s_nop 1\n\t"

        :: "{v7}"(lane16), "{s[16:19]}"(srd), "{s[30:31]}"(wkv), "{s[32:33]}"(wo), "{s34}"((wid - 1) * 6144), "{s36}"(wid), "{s37}"(FLAGS + 24 * FSTR + 16),
           "{v209}"(vkv), "{v210}"(vo), "{v217}"(pn), "{v220}"(lane16)
        : "v8", "v9", "v10", "v11", "v48", "v49", "v50", "v51", "v52", "v53", "v54", "v55", "v56", "v57", "v58", "v59", "v60", "v61", "v62", "v63", "v64", "v65", "v66", "v67", "v68", "v69", "v70", "v71", "v72", "v73", "v74", "v75", "v76", "v77", "v78", "v79", "v80", "v81", "v82", "v83", "v84", "v85", "v86", "v87", "v88", "v89", "v90", "v91", "v92", "v93", "v94", "v95", "v96", "v97", "v98", "v99", "v100", "v101", "v102", "v103", "v104", "v105", "v106", "v107", "v108", "v109", "v110", "v111", "v112", "v113", "v114", "v115", "v116", "v117", "v118", "v119", "v120", "v121", "v122", "v123", "v124", "v125", "v126", "v127", "v128", "v129", "v130", "v131", "v132", "v133", "v134", "v135", "v136", "v137", "v138", "v139", "v140", "v141", "v142", "v143", "v144", "v145", "v146", "v147", "v148", "v149", "v150", "v151", "v152", "v153", "v154", "v155", "v156", "v157", "v158", "v159", "v222", "v223", "v224", "v225", "a172", "a173", "a174", "a175", "a176", "a177", "a178", "a179", "a180", "a181", "a182", "a183", "a184", "a185", "a186", "a187", "a188", "a189", "a190", "a191", "a192", "a193", "a194", "a195", "a196", "a197", "a198", "a199", "a200", "a201", "a202", "a203", "a204", "a205", "a206", "a207", "a208", "a209", "a210", "a211", "a212", "a213", "a214", "a215", "a216", "a217", "a218", "a219", "a220", "a221", "a222", "a223", "a224", "a225", "a226", "a227", "a228", "a229", "a230", "a231", "a232", "a233", "a234", "a235", "a236", "a237", "a238", "a239", "a240", "a241", "a242", "a243", "scc", "memory");
        return;
    }
    {
        asm volatile(
        "s_nop 4\n\t"
    "global_load_dwordx4 a[0:3], v5, s[26:27] offset:0\n\t"
    "global_load_dwordx4 a[4:7], v5, s[26:27] offset:64\n\t"
    "global_load_dwordx4 a[8:11], v5, s[26:27] offset:128\n\t"
    "v_add_u32 v5, 86016, v5\n\t"
    "global_load_dwordx4 a[12:15], v5, s[26:27] offset:0\n\t"
    "global_load_dwordx4 a[16:19], v5, s[26:27] offset:64\n\t"
    "global_load_dwordx4 a[20:23], v5, s[26:27] offset:128\n\t"
    "v_add_u32 v5, 86016, v5\n\t"
    "global_load_dwordx4 a[24:27], v5, s[26:27] offset:0\n\t"
    "global_load_dwordx4 a[28:31], v5, s[26:27] offset:64\n\t"
    "global_load_dwordx4 a[32:35], v5, s[26:27] offset:128\n\t"
    "v_add_u32 v5, 86016, v5\n\t"
    "global_load_dwordx4 a[36:39], v5, s[26:27] offset:0\n\t"
    "global_load_dwordx4 a[40:43], v5, s[26:27] offset:64\n\t"
    "global_load_dwordx4 a[44:47], v5, s[26:27] offset:128\n\t"
    "s_cmp_eq_u32 s36, 0\n\t"
    "s_cbranch_scc0 3f\n\t"
    "v_mov_b32 v222, 0x7f000\n\t"
    "global_load_dword v221, v222, s[16:17] sc1\n\t"
    "3:\n\t"

        : "+{v5}"(qa2), "+v"(lane)
        : "{v7}"(lane16), "{s[16:19]}"(srd), "{s[26:27]}"(x), "{s36}"(wid)
        : "a0", "a1", "a2", "a3", "a4", "a5", "a6", "a7", "a8", "a9", "a10", "a11", "a12", "a13", "a14", "a15", "a16", "a17", "a18", "a19", "a20", "a21", "a22", "a23", "a24", "a25", "a26", "a27", "a28", "a29", "a30", "a31", "a32", "a33", "a34", "a35", "a36", "a37", "a38", "a39", "a40", "a41", "a42", "a43", "a44", "a45", "a46", "a47", "v221", "v222", "v223", "v224", "s48", "s49", "scc", "memory");
    }
    const int l15 = lane & 15, g = lane >> 4;
    const unsigned ga = (wid < 2 ? ((l15 >> 2) * 16 + g * 4 + (l15 & 3)) * 16 : lane * 16) + wid * 3072;
    const unsigned prow = 32 * (wid - 1) + 8 * (l15 >> 2) + (l15 & 3), vq = prow * 192 + g * 16, vkv = prow * 384 + g * 16;
    const unsigned ones = (l15 & 3) == 0 ? 0x3f803f80u : 0u;
    const int c0 = 32 * (g & 1) + 2 * l15 + (g >> 1), c1 = 64 + 2 * l15 + (g >> 1);
    unsigned xw0 = XS + wid * 192 + c0 * 2, xw1 = XS + wid * 192 + c1 * 2;
    unsigned xt0 = XT + (2 * (c0 >> 5) + ((c0 >> 2) & 1)) * 128 + (4 * ((c0 >> 3) & 3) + (c0 & 3)) * 8 + wid * 2, xt1 = xt0 + (2 - (g & 1)) * 256;
    unsigned xr = XS + (l15 & 3) * 192 + g * 16, pr = XS + (lane & 3) * 192 + (lane >> 2) * 16;
    const int tile = blk >> 3, kh = (blk >> 2) & 1, j = blk & 3;
    unsigned pk = tile * TILEB + (kh * 3 + g) * 1024 + j * 256 + l15 * 16;
    unsigned pv = tile * TILEB + 6144 + (lane >> 3) * 1024 + kh * 512 + j * 128 + (lane & 7) * 16;
    const int pl = lane < 49 ? lane : 49;
    unsigned fa = FLAGS + (pl >> 1) * FSTR + (pl & 1) * 16 + (blk % NREP) * 8192, wb = lane * 16 + (wid - 1) * 6144;
    u32x4 sel0, sel1;
#pragma unroll
    for (int r = 0; r < 4; ++r) { const bool on = g == (l15 >> 2) && r == (l15 & 3); sel0[r] = on ? 0x00003c80u : 0u; sel1[r] = on ? 0x3c800000u : 0u; }
    const unsigned woff = wid * 3072;
    const int zf = RFL(tile == NTILE - 1 ? 1 : 0);
    unsigned la = lane * 16, lb = lane * 8, oa = qa, s20 = 0, s22 = woff, epoch;
    unsigned bad;
    const unsigned park = (4u << 20) + (blk * 4 + wid) * 32768u;
    asm volatile(
    "s_nop 4\n\t"
    "s_cmp_eq_u32 s36, 0\n\t"
    "s_cbranch_scc1 3f\n\t"
    "s_add_u32 m0, s34, 0\n\t"
    "s_nop 0\n\t"
    "global_load_lds_dwordx4 v208, s[28:29] offset:0\n\t"
    "s_add_u32 m0, s34, 960\n\t"
    "s_nop 0\n\t"
    "global_load_lds_dwordx4 v208, s[28:29] offset:64\n\t"
    "s_add_u32 m0, s34, 1920\n\t"
    "s_nop 0\n\t"
    "global_load_lds_dwordx4 v208, s[28:29] offset:128\n\t"
    "s_add_u32 m0, s34, 2304\n\t"
    "s_nop 0\n\t"
    "global_load_lds_dwordx4 v208, s[28:29] offset:768\n\t"
    "s_add_u32 m0, s34, 3264\n\t"
    "s_nop 0\n\t"
    "global_load_lds_dwordx4 v208, s[28:29] offset:832\n\t"
    "s_add_u32 m0, s34, 4224\n\t"
    "s_nop 0\n\t"
    "global_load_lds_dwordx4 v208, s[28:29] offset:896\n\t"
    "s_add_u32 m0, s34, 18432\n\t"
    "s_nop 0\n\t"
    "global_load_lds_dwordx4 v209, s[30:31] offset:0\n\t"
    "s_add_u32 m0, s34, 19392\n\t"
    "s_nop 0\n\t"
    "global_load_lds_dwordx4 v209, s[30:31] offset:64\n\t"
    "s_add_u32 m0, s34, 20352\n\t"
    "s_nop 0\n\t"
    "global_load_lds_dwordx4 v209, s[30:31] offset:128\n\t"
    "s_add_u32 m0, s34, 19968\n\t"
    "s_nop 0\n\t"
    "global_load_lds_dwordx4 v209, s[30:31] offset:1536\n\t"
    "s_add_u32 m0, s34, 20928\n\t"
    "s_nop 0\n\t"
    "global_load_lds_dwordx4 v209, s[30:31] offset:1600\n\t"
    "s_add_u32 m0, s34, 21888\n\t"
    "s_nop 0\n\t"
    "global_load_lds_dwordx4 v209, s[30:31] offset:1664\n\t"
    "3:\n\t"
    "v_accvgpr_write_b32 a96, v6\n\t"
    "v_accvgpr_write_b32 a97, v6\n\t"
    "v_accvgpr_write_b32 a98, v6\n\t"
    "v_accvgpr_write_b32 a99, v6\n\t"
    "v_mov_b32 v225, 98304\n\t"
    "v_mov_b32 v232, 0\n\t"
    "v_lshl_add_u32 v223, s36, 2, v225\n\t"
    "s_mov_b64 exec, 1\n\t"
    "ds_write_b32 v223, v232\n\t"
    "s_mov_b64 exec, -1\n\t"
    "s_mov_b32 s23, 49152\n\t"
    "s_mov_b32 s24, 5\n\t"
    "s_cmp_eq_u32 s36, 0\n\t"
    "s_cbranch_scc1 2f\n\t"
    "s_waitcnt vmcnt(21)\n\t"
    "s_branch 3f\n\t"
    "2:\n\t"
    "s_waitcnt vmcnt(10)\n\t"
    "3:\n\t"
    "v_mfma_f32_16x16x32_bf16 v[48:51], a[0:3], v[8:11], 0\n\t"
    "v_mfma_f32_16x16x32_bf16 v[52:55], a[0:3], v[12:15], 0\n\t"
    "v_mfma_f32_16x16x32_bf16 v[56:59], a[4:7], v[8:11], 0\n\t"
    "v_mfma_f32_16x16x32_bf16 v[60:63], a[4:7], v[12:15], 0\n\t"
    "v_mfma_f32_16x16x32_bf16 v[64:67], a[8:11], v[8:11], 0\n\t"
    "v_mfma_f32_16x16x32_bf16 v[68:71], a[8:11], v[12:15], 0\n\t"
    "s_cbranch_scc1 2f\n\t"
    "s_waitcnt vmcnt(18)\n\t"
    "s_branch 3f\n\t"
    "2:\n\t"
    "s_waitcnt vmcnt(7)\n\t"
    "3:\n\t"
    "v_mfma_f32_16x16x32_bf16 v[48:51], a[12:15], v[8:11], v[48:51]\n\t"
    "v_mfma_f32_16x16x32_bf16 v[52:55], a[12:15], v[12:15], v[52:55]\n\t"
    "v_mfma_f32_16x16x32_bf16 v[56:59], a[16:19], v[8:11], v[56:59]\n\t"
    "v_mfma_f32_16x16x32_bf16 v[60:63], a[16:19], v[12:15], v[60:63]\n\t"
    "v_mfma_f32_16x16x32_bf16 v[64:67], a[20:23], v[8:11], v[64:67]\n\t"
    "v_mfma_f32_16x16x32_bf16 v[68:71], a[20:23], v[12:15], v[68:71]\n\t"
    "s_cbranch_scc1 2f\n\t"
    "s_waitcnt vmcnt(15)\n\t"
    "s_branch 3f\n\t"
    "2:\n\t"
    "s_waitcnt vmcnt(4)\n\t"
    "3:\n\t"
    "v_mfma_f32_16x16x32_bf16 v[48:51], a[24:27], v[8:11], v[48:51]\n\t"
    "v_mfma_f32_16x16x32_bf16 v[52:55], a[24:27], v[12:15], v[52:55]\n\t"
    "v_mfma_f32_16x16x32_bf16 v[56:59], a[28:31], v[8:11], v[56:59]\n\t"
    "v_mfma_f32_16x16x32_bf16 v[60:63], a[28:31], v[12:15], v[60:63]\n\t"
    "v_mfma_f32_16x16x32_bf16 v[64:67], a[32:35], v[8:11], v[64:67]\n\t"
    "v_mfma_f32_16x16x32_bf16 v[68:71], a[32:35], v[12:15], v[68:71]\n\t"
    "s_cbranch_scc1 2f\n\t"
    "s_waitcnt vmcnt(12)\n\t"
    "s_branch 3f\n\t"
    "2:\n\t"
    "s_waitcnt vmcnt(1)\n\t"
    "3:\n\t"
    "v_mfma_f32_16x16x32_bf16 v[48:51], a[36:39], v[8:11], v[48:51]\n\t"
    "v_mfma_f32_16x16x32_bf16 v[52:55], a[36:39], v[12:15], v[52:55]\n\t"
    "v_mfma_f32_16x16x32_bf16 v[56:59], a[40:43], v[8:11], v[56:59]\n\t"
    "v_mfma_f32_16x16x32_bf16 v[60:63], a[40:43], v[12:15], v[60:63]\n\t"
    "v_mfma_f32_16x16x32_bf16 v[64:67], a[44:47], v[8:11], v[64:67]\n\t"
    "v_mfma_f32_16x16x32_bf16 v[68:71], a[44:47], v[12:15], v[68:71]\n\t"
    "s_nop 2\n\t"
    "v_add_f32 v48, v48, v49\n\t"
    "v_add_f32 v50, v50, v51\n\t"
    "v_add_f32 v52, v52, v53\n\t"
    "v_add_f32 v54, v54, v55\n\t"
    "v_add_f32 v56, v56, v57\n\t"
    "v_add_f32 v58, v58, v59\n\t"
    "v_add_f32 v60, v60, v61\n\t"
    "v_add_f32 v62, v62, v63\n\t"
    "v_add_f32 v64, v64, v65\n\t"
    "v_add_f32 v66, v66, v67\n\t"
    "v_add_f32 v68, v68, v69\n\t"
    "v_add_f32 v70, v70, v71\n\t"
    "v_add_f32 v48, v48, v50\n\t"
    "v_add_f32 v52, v52, v54\n\t"
    "v_add_f32 v56, v56, v58\n\t"
    "v_add_f32 v60, v60, v62\n\t"
    "v_add_f32 v64, v64, v66\n\t"
    "v_add_f32 v68, v68, v70\n\t"
    "v_permlane32_swap_b32 v48, v52\n\t"
    "v_permlane32_swap_b32 v56, v60\n\t"
    "v_permlane32_swap_b32 v64, v68\n\t"
    "v_add_f32 v64, v64, v68\n\t"
    "v_add_f32 v48, v48, v52\n\t"
    "v_add_f32 v56, v56, v60\n\t"
    "v_mov_b32 v65, v64\n\t"
    "s_nop 0\n\t"
    "v_permlane16_swap_b32 v48, v56\n\t"
    "v_permlane16_swap_b32 v64, v65\n\t"
    "v_add_f32 v48, v48, v56\n\t"
    "v_add_f32 v64, v64, v65\n\t"
    "v_cvt_pk_bf16_f32 v56, v48, v64\n\t"
    "v_mul_f32 v48, 0x3e16c740, v48\n\t"
    "v_mul_f32 v64, 0x3e16c740, v64\n\t"
    "v_cvt_pk_bf16_f32 v48, v48, v64\n\t"
    "ds_write_b16 v213, v56\n\t"
    "ds_write_b16_d16_hi v214, v56\n\t"
    "ds_write_b16 v211, v48\n\t"
    "ds_write_b16_d16_hi v212, v48\n\t"
    "s_waitcnt lgkmcnt(0)\n\t"
    "s_barrier\n\t"
    "s_cbranch_scc0 2f\n\t"
    "ds_read_b128 v[80:83], v216 offset:0\n\t"
    "v_add_u32 v222, 136192, v7\n\t"
    "ds_read_b128 v[84:87], v222 offset:0\n\t"
    "s_mov_b32 exec_hi, 0xffff\n\t"
    "s_waitcnt lgkmcnt(1)\n\t"
    "global_store_dwordx4 v217, v[80:83], s[16:17] sc1\n\t"
    "s_waitcnt lgkmcnt(0)\n\t"
    "global_store_dwordx4 v218, v[84:87], s[16:17] sc1\n\t"
    "s_cmp_eq_u32 s38, 0\n\t"
    "s_cbranch_scc1 4f\n\t"
    "v_mov_b32 v88, 0\n\t"
    "v_mov_b32 v89, 0\n\t"
    "v_mov_b32 v90, 0\n\t"
    "v_mov_b32 v91, 0\n\t"
    "global_store_dwordx4 v218, v[88:91], s[16:17] offset:512 sc1\n\t"
    "4:\n\t"
    "v_lshl_add_u32 v222, v7, 9, s37\n\t"
    "s_mov_b64 exec, -1\n\t"
    "s_waitcnt vmcnt(0)\n\t"
    "v_add_u32 v221, 1, v221\n\t"
    "s_mov_b64 exec, 255\n\t"
    "global_store_dword v222, v221, s[16:17] sc1\n\t"
    "s_mov_b64 exec, -1\n\t"
    "v_readfirstlane_b32 s45, v221\n\t"
    "s_branch 3f\n\t"
    "2:\n\t"
    "v_mov_b32 v232, 1\n\t"
    "s_mov_b64 exec, 1\n\t"
    "s_waitcnt vmcnt(0)\n\t"
    "ds_write_b32 v223, v232\n\t"
    "7:\n\t"
    "ds_read_b128 v[228:231], v225 offset:0\n\t"
    "s_waitcnt lgkmcnt(0)\n\t"
    "v_and_b32 v229, v229, v230\n\t"
    "v_and_b32 v229, v229, v231\n\t"
    "v_cmp_eq_u32 vcc, 1, v229\n\t"
    "s_nop 0\n\t"
    "s_cmp_eq_u64 vcc, 0\n\t"
    "s_cbranch_scc1 7b\n\t"
    "s_mov_b64 exec, -1\n\t"
    "ds_read_b128 a[172:175], v7 offset:0\n\t"
    "ds_read_b128 v[48:51], v220 offset:18432\n\t"
    "ds_read_b128 v[60:63], v220 offset:21504\n\t"
    "ds_read_b128 a[184:187], v7 offset:3072\n\t"
    "ds_read_b128 a[176:179], v7 offset:1024\n\t"
    "ds_read_b128 v[52:55], v220 offset:19456\n\t"
    "ds_read_b128 v[64:67], v220 offset:22528\n\t"
    "ds_read_b128 a[188:191], v7 offset:4096\n\t"
    "ds_read_b128 a[180:183], v7 offset:2048\n\t"
    "ds_read_b128 v[56:59], v220 offset:20480\n\t"
    "ds_read_b128 v[68:71], v220 offset:23552\n\t"
    "ds_read_b128 a[192:195], v7 offset:5120\n\t"
    "s_waitcnt lgkmcnt(9)\n\t"
    "v_mfma_f32_16x16x32_bf16 v[72:75], a[172:175], v[48:51], 0\n\t"
    "ds_read_b128 a[196:199], v7 offset:6144\n\t"
    "ds_read_b128 a[208:211], v7 offset:9216\n\t"
    "v_mfma_f32_16x16x32_bf16 v[96:99], a[172:175], v[60:63], 0\n\t"
    "s_waitcnt lgkmcnt(10)\n\t"
    "ds_read_b128 a[200:203], v7 offset:7168\n\t"
    "ds_read_b128 a[212:215], v7 offset:10240\n\t"
    "v_mfma_f32_16x16x32_bf16 v[76:79], a[184:187], v[48:51], 0\n\t"
    "ds_read_b128 a[204:207], v7 offset:8192\n\t"
    "ds_read_b128 a[216:219], v7 offset:11264\n\t"
    "v_mfma_f32_16x16x32_bf16 v[100:103], a[184:187], v[60:63], 0\n\t"
    "s_waitcnt lgkmcnt(10)\n\t"
    "ds_read_b128 a[220:223], v7 offset:12288\n\t"
    "ds_read_b128 a[232:235], v7 offset:15360\n\t"
    "v_mfma_f32_16x16x32_bf16 v[72:75], a[176:179], v[52:55], v[72:75]\n\t"
    "ds_read_b128 a[224:227], v7 offset:13312\n\t"
    "ds_read_b128 a[236:239], v7 offset:16384\n\t"
    "v_mfma_f32_16x16x32_bf16 v[96:99], a[176:179], v[64:67], v[96:99]\n\t"
    "ds_read_b128 a[228:231], v7 offset:14336\n\t"
    "ds_read_b128 a[240:243], v7 offset:17408\n\t"
    "v_mfma_f32_16x16x32_bf16 v[76:79], a[188:191], v[52:55], v[76:79]\n\t"
    "v_mfma_f32_16x16x32_bf16 v[100:103], a[188:191], v[64:67], v[100:103]\n\t"
    "s_waitcnt lgkmcnt(12)\n\t"
    "v_mfma_f32_16x16x32_bf16 v[72:75], a[180:183], v[56:59], v[72:75]\n\t"
    "v_mfma_f32_16x16x32_bf16 v[96:99], a[180:183], v[68:71], v[96:99]\n\t"
    "v_mfma_f32_16x16x32_bf16 v[76:79], a[192:195], v[56:59], v[76:79]\n\t"
    "v_mfma_f32_16x16x32_bf16 v[100:103], a[192:195], v[68:71], v[100:103]\n\t"
    "s_waitcnt lgkmcnt(10)\n\t"
    "v_mfma_f32_16x16x32_bf16 v[80:83], a[196:199], v[48:51], 0\n\t"
    "s_nop 2\n\t"
    "v_cvt_pk_bf16_f32 v72, v72, v73\n\t"
    "v_cvt_pk_bf16_f32 v73, v74, v75\n\t"
    "v_mfma_f32_16x16x32_bf16 v[104:107], a[196:199], v[60:63], 0\n\t"
    "v_cvt_pk_bf16_f32 v74, v76, v77\n\t"
    "v_cvt_pk_bf16_f32 v75, v78, v79\n\t"
    "v_mfma_f32_16x16x32_bf16 v[84:87], a[208:211], v[48:51], 0\n\t"
    "v_cvt_pk_bf16_f32 v96, v96, v97\n\t"
    "v_cvt_pk_bf16_f32 v97, v98, v99\n\t"
    "v_mfma_f32_16x16x32_bf16 v[108:111], a[208:211], v[60:63], 0\n\t"
    "s_waitcnt lgkmcnt(8)\n\t"
    "v_cvt_pk_bf16_f32 v98, v100, v101\n\t"
    "v_cvt_pk_bf16_f32 v99, v102, v103\n\t"
    "v_mfma_f32_16x16x32_bf16 v[80:83], a[200:203], v[52:55], v[80:83]\n\t"
    "ds_write_b128 v220, v[72:75] offset:36864\n\t"
    "ds_write_b128 v220, v[96:99] offset:39936\n\t"
    "v_mfma_f32_16x16x32_bf16 v[104:107], a[200:203], v[64:67], v[104:107]\n\t"
    "v_mfma_f32_16x16x32_bf16 v[84:87], a[212:215], v[52:55], v[84:87]\n\t"
    "v_mfma_f32_16x16x32_bf16 v[108:111], a[212:215], v[64:67], v[108:111]\n\t"
    "s_waitcnt lgkmcnt(8)\n\t"
    "v_mfma_f32_16x16x32_bf16 v[80:83], a[204:207], v[56:59], v[80:83]\n\t"
    "v_mfma_f32_16x16x32_bf16 v[104:107], a[204:207], v[68:71], v[104:107]\n\t"
    "v_mfma_f32_16x16x32_bf16 v[84:87], a[216:219], v[56:59], v[84:87]\n\t"
    "v_mfma_f32_16x16x32_bf16 v[108:111], a[216:219], v[68:71], v[108:111]\n\t"
    "s_waitcnt lgkmcnt(6)\n\t"
    "v_mfma_f32_16x16x32_bf16 v[88:91], a[220:223], v[48:51], 0\n\t"
    "s_nop 2\n\t"
    "v_cvt_pk_bf16_f32 v80, v80, v81\n\t"
    "v_cvt_pk_bf16_f32 v81, v82, v83\n\t"
    "v_mfma_f32_16x16x32_bf16 v[112:115], a[220:223], v[60:63], 0\n\t"
    "v_cvt_pk_bf16_f32 v82, v84, v85\n\t"
    "v_cvt_pk_bf16_f32 v83, v86, v87\n\t"
    "v_mfma_f32_16x16x32_bf16 v[92:95], a[232:235], v[48:51], 0\n\t"
    "v_cvt_pk_bf16_f32 v104, v104, v105\n\t"
    "v_cvt_pk_bf16_f32 v105, v106, v107\n\t"
    "v_mfma_f32_16x16x32_bf16 v[116:119], a[232:235], v[60:63], 0\n\t"
    "s_waitcnt lgkmcnt(4)\n\t"
    "v_cvt_pk_bf16_f32 v106, v108, v109\n\t"
    "v_cvt_pk_bf16_f32 v107, v110, v111\n\t"
    "v_mfma_f32_16x16x32_bf16 v[88:91], a[224:227], v[52:55], v[88:91]\n\t"
    "ds_write_b128 v220, v[80:83] offset:37888\n\t"
    "ds_write_b128 v220, v[104:107] offset:40960\n\t"
    "v_mfma_f32_16x16x32_bf16 v[112:115], a[224:227], v[64:67], v[112:115]\n\t"
    "v_mfma_f32_16x16x32_bf16 v[92:95], a[236:239], v[52:55], v[92:95]\n\t"
    "v_mfma_f32_16x16x32_bf16 v[116:119], a[236:239], v[64:67], v[116:119]\n\t"
    "s_waitcnt lgkmcnt(4)\n\t"
    "v_mfma_f32_16x16x32_bf16 v[88:91], a[228:231], v[56:59], v[88:91]\n\t"
    "v_mfma_f32_16x16x32_bf16 v[112:115], a[228:231], v[68:71], v[112:115]\n\t"
    "v_mfma_f32_16x16x32_bf16 v[92:95], a[240:243], v[56:59], v[92:95]\n\t"
    "v_mfma_f32_16x16x32_bf16 v[116:119], a[240:243], v[68:71], v[116:119]\n\t"
    "s_nop 4\n\t"
    "v_cvt_pk_bf16_f32 v88, v88, v89\n\t"
    "v_cvt_pk_bf16_f32 v89, v90, v91\n\t"
    "v_cvt_pk_bf16_f32 v90, v92, v93\n\t"
    "v_cvt_pk_bf16_f32 v91, v94, v95\n\t"
    "v_cvt_pk_bf16_f32 v112, v112, v113\n\t"
    "v_cvt_pk_bf16_f32 v113, v114, v115\n\t"
    "v_cvt_pk_bf16_f32 v114, v116, v117\n\t"
    "v_cvt_pk_bf16_f32 v115, v118, v119\n\t"
    "ds_write_b128 v220, v[88:91] offset:38912\n\t"
    "ds_write_b128 v220, v[112:115] offset:41984\n\t"
    "3:\n\t"
    "s_waitcnt lgkmcnt(0)\n\t"
    "s_barrier\n\t"
    "ds_read_b128 a[172:175], v7 offset:36864\n\t"
    "ds_read_b128 a[184:187], v7 offset:39936\n\t"
    "ds_read_b128 a[196:199], v7 offset:43008\n\t"
    "ds_read_b128 a[208:211], v7 offset:46080\n\t"
    "ds_read_b128 a[220:223], v7 offset:49152\n\t"
    "ds_read_b128 a[232:235], v7 offset:52224\n\t"
    "ds_read_b128 a[176:179], v7 offset:37888\n\t"
    "ds_read_b128 a[188:191], v7 offset:40960\n\t"
    "s_waitcnt lgkmcnt(7)\n\t"
    "v_mfma_f32_16x16x32_bf16 v[48:51], a[172:175], a[0:3], 0\n\t"
    "ds_read_b128 a[200:203], v7 offset:44032\n\t"
    "s_waitcnt lgkmcnt(7)\n\t"
    "v_mfma_f32_16x16x32_bf16 v[52:55], a[184:187], a[0:3], 0\n\t"
    "ds_read_b128 a[212:215], v7 offset:47104\n\t"
    "s_waitcnt lgkmcnt(7)\n\t"
    "v_mfma_f32_16x16x32_bf16 v[56:59], a[196:199], a[0:3], 0\n\t"
    "ds_read_b128 a[224:227], v7 offset:50176\n\t"
    "s_waitcnt lgkmcnt(7)\n\t"
    "v_mfma_f32_16x16x32_bf16 v[60:63], a[208:211], a[0:3], 0\n\t"
    "ds_read_b128 a[236:239], v7 offset:53248\n\t"
    "s_waitcnt lgkmcnt(7)\n\t"
    "v_mfma_f32_16x16x32_bf16 v[64:67], a[220:223], a[0:3], 0\n\t"
    "ds_read_b128 a[180:183], v7 offset:38912\n\t"
    "s_waitcnt lgkmcnt(7)\n\t"
    "v_mfma_f32_16x16x32_bf16 v[68:71], a[232:235], a[0:3], 0\n\t"
    "ds_read_b128 a[192:195], v7 offset:41984\n\t"
    "s_waitcnt lgkmcnt(7)\n\t"
    "v_mfma_f32_16x16x32_bf16 v[48:51], a[176:179], a[4:7], v[48:51]\n\t"
    "ds_read_b128 a[204:207], v7 offset:45056\n\t"
    "s_waitcnt lgkmcnt(7)\n\t"
    "v_mfma_f32_16x16x32_bf16 v[52:55], a[188:191], a[4:7], v[52:55]\n\t"
    "ds_read_b128 a[216:219], v7 offset:48128\n\t"
    "s_waitcnt lgkmcnt(7)\n\t"
    "v_mfma_f32_16x16x32_bf16 v[56:59], a[200:203], a[4:7], v[56:59]\n\t"
    "ds_read_b128 a[228:231], v7 offset:51200\n\t"
    "s_waitcnt lgkmcnt(7)\n\t"
    "v_mfma_f32_16x16x32_bf16 v[60:63], a[212:215], a[4:7], v[60:63]\n\t"
    "ds_read_b128 a[240:243], v7 offset:54272\n\t"
    "s_waitcnt lgkmcnt(7)\n\t"
    "v_mfma_f32_16x16x32_bf16 v[64:67], a[224:227], a[4:7], v[64:67]\n\t"
    "ds_read_b128 v[144:147], v215 offset:0\n\t"
    "s_waitcnt lgkmcnt(7)\n\t"
    "v_mfma_f32_16x16x32_bf16 v[68:71], a[236:239], a[4:7], v[68:71]\n\t"
    "ds_read_b128 v[148:151], v215 offset:64\n\t"
    "s_waitcnt lgkmcnt(7)\n\t"
    "v_mfma_f32_16x16x32_bf16 v[48:51], a[180:183], a[8:11], v[48:51]\n\t"
    "ds_read_b128 v[152:155], v215 offset:128\n\t"
    "s_waitcnt lgkmcnt(7)\n\t"
    "v_mfma_f32_16x16x32_bf16 v[52:55], a[192:195], a[8:11], v[52:55]\n\t"
    "s_waitcnt lgkmcnt(6)\n\t"
    "v_mfma_f32_16x16x32_bf16 v[56:59], a[204:207], a[8:11], v[56:59]\n\t"
    "s_waitcnt lgkmcnt(5)\n\t"
    "v_mfma_f32_16x16x32_bf16 v[60:63], a[216:219], a[8:11], v[60:63]\n\t"
    "s_waitcnt lgkmcnt(4)\n\t"
    "v_mfma_f32_16x16x32_bf16 v[64:67], a[228:231], a[8:11], v[64:67]\n\t"
    "v_cvt_pk_bf16_f32 v160, v48, v49\n\t"
    "v_cvt_pk_bf16_f32 v161, v50, v51\n\t"
    "s_waitcnt lgkmcnt(3)\n\t"
    "v_mfma_f32_16x16x32_bf16 v[68:71], a[240:243], a[8:11], v[68:71]\n\t"
    "v_cvt_pk_bf16_f32 v162, v52, v53\n\t"
    "v_cvt_pk_bf16_f32 v163, v54, v55\n\t"
    "v_mfma_f32_16x16x32_bf16 v[72:75], a[172:175], a[12:15], 0\n\t"
    "v_cvt_pk_bf16_f32 v164, v56, v57\n\t"
    "v_cvt_pk_bf16_f32 v165, v58, v59\n\t"
    "v_mfma_f32_16x16x32_bf16 v[76:79], a[184:187], a[12:15], 0\n\t"
    "v_cvt_pk_bf16_f32 v166, v60, v61\n\t"
    "v_cvt_pk_bf16_f32 v167, v62, v63\n\t"
    "v_mfma_f32_16x16x32_bf16 v[80:83], a[196:199], a[12:15], 0\n\t"
    "v_cvt_pk_bf16_f32 v168, v64, v65\n\t"
    "v_cvt_pk_bf16_f32 v169, v66, v67\n\t"
    "v_mfma_f32_16x16x32_bf16 v[84:87], a[208:211], a[12:15], 0\n\t"
    "v_cvt_pk_bf16_f32 v170, v68, v69\n\t"
    "v_cvt_pk_bf16_f32 v171, v70, v71\n\t"
    "v_mfma_f32_16x16x32_bf16 v[88:91], a[220:223], a[12:15], 0\n\t"
    "v_mfma_f32_16x16x32_bf16 v[92:95], a[232:235], a[12:15], 0\n\t"
    "v_mfma_f32_16x16x32_bf16 v[72:75], a[176:179], a[16:19], v[72:75]\n\t"
    "v_mfma_f32_16x16x32_bf16 v[76:79], a[188:191], a[16:19], v[76:79]\n\t"
    "v_mfma_f32_16x16x32_bf16 v[80:83], a[200:203], a[16:19], v[80:83]\n\t"
    "v_mfma_f32_16x16x32_bf16 v[84:87], a[212:215], a[16:19], v[84:87]\n\t"
    "v_mfma_f32_16x16x32_bf16 v[88:91], a[224:227], a[16:19], v[88:91]\n\t"
    "v_mfma_f32_16x16x32_bf16 v[92:95], a[236:239], a[16:19], v[92:95]\n\t"
    "v_mfma_f32_16x16x32_bf16 v[72:75], a[180:183], a[20:23], v[72:75]\n\t"
    "v_mfma_f32_16x16x32_bf16 v[76:79], a[192:195], a[20:23], v[76:79]\n\t"
    "v_mfma_f32_16x16x32_bf16 v[80:83], a[204:207], a[20:23], v[80:83]\n\t"
    "v_mfma_f32_16x16x32_bf16 v[84:87], a[216:219], a[20:23], v[84:87]\n\t"
    "v_mfma_f32_16x16x32_bf16 v[88:91], a[228:231], a[20:23], v[88:91]\n\t"
    "s_nop 3\n\t"
    "v_cvt_pk_bf16_f32 v172, v72, v73\n\t"
    "v_cvt_pk_bf16_f32 v173, v74, v75\n\t"
    "v_mfma_f32_16x16x32_bf16 v[92:95], a[240:243], a[20:23], v[92:95]\n\t"
    "v_cvt_pk_bf16_f32 v174, v76, v77\n\t"
    "v_cvt_pk_bf16_f32 v175, v78, v79\n\t"
    "v_mfma_f32_16x16x32_bf16 v[48:51], a[172:175], a[24:27], 0\n\t"
    "v_cvt_pk_bf16_f32 v176, v80, v81\n\t"
    "v_cvt_pk_bf16_f32 v177, v82, v83\n\t"
    "v_mfma_f32_16x16x32_bf16 v[52:55], a[184:187], a[24:27], 0\n\t"
    "v_cvt_pk_bf16_f32 v178, v84, v85\n\t"
    "v_cvt_pk_bf16_f32 v179, v86, v87\n\t"
    "v_mfma_f32_16x16x32_bf16 v[56:59], a[196:199], a[24:27], 0\n\t"
    "v_cvt_pk_bf16_f32 v180, v88, v89\n\t"
    "v_cvt_pk_bf16_f32 v181, v90, v91\n\t"
    "v_mfma_f32_16x16x32_bf16 v[60:63], a[208:211], a[24:27], 0\n\t"
    "v_cvt_pk_bf16_f32 v182, v92, v93\n\t"
    "v_cvt_pk_bf16_f32 v183, v94, v95\n\t"
    "v_mfma_f32_16x16x32_bf16 v[64:67], a[220:223], a[24:27], 0\n\t"
    "v_mfma_f32_16x16x32_bf16 v[68:71], a[232:235], a[24:27], 0\n\t"
    "v_mfma_f32_16x16x32_bf16 v[48:51], a[176:179], a[28:31], v[48:51]\n\t"
    "v_mfma_f32_16x16x32_bf16 v[52:55], a[188:191], a[28:31], v[52:55]\n\t"
    "v_mfma_f32_16x16x32_bf16 v[56:59], a[200:203], a[28:31], v[56:59]\n\t"
    "v_mfma_f32_16x16x32_bf16 v[60:63], a[212:215], a[28:31], v[60:63]\n\t"
    "v_mfma_f32_16x16x32_bf16 v[64:67], a[224:227], a[28:31], v[64:67]\n\t"
    "v_mfma_f32_16x16x32_bf16 v[68:71], a[236:239], a[28:31], v[68:71]\n\t"
    "v_mfma_f32_16x16x32_bf16 v[48:51], a[180:183], a[32:35], v[48:51]\n\t"
    "v_mfma_f32_16x16x32_bf16 v[52:55], a[192:195], a[32:35], v[52:55]\n\t"
    "v_mfma_f32_16x16x32_bf16 v[56:59], a[204:207], a[32:35], v[56:59]\n\t"
    "v_mfma_f32_16x16x32_bf16 v[60:63], a[216:219], a[32:35], v[60:63]\n\t"
    "v_mfma_f32_16x16x32_bf16 v[64:67], a[228:231], a[32:35], v[64:67]\n\t"
    "s_nop 3\n\t"
    "v_cvt_pk_bf16_f32 v184, v48, v49\n\t"
    "v_cvt_pk_bf16_f32 v185, v50, v51\n\t"
    "v_mfma_f32_16x16x32_bf16 v[68:71], a[240:243], a[32:35], v[68:71]\n\t"
    "v_cvt_pk_bf16_f32 v186, v52, v53\n\t"
    "v_cvt_pk_bf16_f32 v187, v54, v55\n\t"
    "v_mfma_f32_16x16x32_bf16 v[72:75], a[172:175], a[36:39], 0\n\t"
    "v_cvt_pk_bf16_f32 v188, v56, v57\n\t"
    "v_cvt_pk_bf16_f32 v189, v58, v59\n\t"
    "v_mfma_f32_16x16x32_bf16 v[76:79], a[184:187], a[36:39], 0\n\t"
    "v_cvt_pk_bf16_f32 v190, v60, v61\n\t"
    "v_cvt_pk_bf16_f32 v191, v62, v63\n\t"
    "v_mfma_f32_16x16x32_bf16 v[80:83], a[196:199], a[36:39], 0\n\t"
    "v_cvt_pk_bf16_f32 v192, v64, v65\n\t"
    "v_cvt_pk_bf16_f32 v193, v66, v67\n\t"
    "v_mfma_f32_16x16x32_bf16 v[84:87], a[208:211], a[36:39], 0\n\t"
    "v_cvt_pk_bf16_f32 v194, v68, v69\n\t"
    "v_cvt_pk_bf16_f32 v195, v70, v71\n\t"
    "v_mfma_f32_16x16x32_bf16 v[88:91], a[220:223], a[36:39], 0\n\t"
    "v_mfma_f32_16x16x32_bf16 v[92:95], a[232:235], a[36:39], 0\n\t"
    "v_mfma_f32_16x16x32_bf16 v[72:75], a[176:179], a[40:43], v[72:75]\n\t"
    "v_mfma_f32_16x16x32_bf16 v[76:79], a[188:191], a[40:43], v[76:79]\n\t"
    "v_mfma_f32_16x16x32_bf16 v[80:83], a[200:203], a[40:43], v[80:83]\n\t"
    "s_cmp_lg_u32 s36, 0\n\t"
    "s_cbranch_scc1 5f\n\t"
    "s_mov_b32 s44, 0x40000\n\t"
    "global_load_dwordx4 v[228:231], v219, s[16:17] sc1\n\t"
    "s_sleep 4\n\t"
    "8:\n\t"
    "global_load_dwordx4 v[232:235], v219, s[16:17] sc1\n\t"
    "s_waitcnt vmcnt(1)\n\t"
    "v_xor_b32 v222, v228, v221\n\t"
    "v_xor_b32 v223, v229, v221\n\t"
    "v_xor_b32 v224, v230, v221\n\t"
    "v_xor_b32 v225, v231, v221\n\t"
    "v_or3_b32 v222, v222, v223, v224\n\t"
    "v_or_b32 v222, v222, v225\n\t"
    "v_cmp_ne_u32 vcc, 0, v222\n\t"
    "s_nop 0\n\t"
    "s_cmp_eq_u64 vcc, 0\n\t"
    "s_cbranch_scc1 6f\n\t"
    "global_load_dwordx4 v[228:231], v219, s[16:17] sc1\n\t"
    "s_waitcnt vmcnt(1)\n\t"
    "v_xor_b32 v222, v232, v221\n\t"
    "v_xor_b32 v223, v233, v221\n\t"
    "v_xor_b32 v224, v234, v221\n\t"
    "v_xor_b32 v225, v235, v221\n\t"
    "v_or3_b32 v222, v222, v223, v224\n\t"
    "v_or_b32 v222, v222, v225\n\t"
    "v_cmp_ne_u32 vcc, 0, v222\n\t"
    "s_nop 0\n\t"
    "s_cmp_eq_u64 vcc, 0\n\t"
    "s_cbranch_scc1 6f\n\t"
    "s_sub_u32 s44, s44, 1\n\t"
    "s_cbranch_scc0 8b\n\t"
    "5:\n\t"
    "6:\n\t"
    "s_barrier\n\t"
    "v_mfma_f32_16x16x32_bf16 v[84:87], a[212:215], a[40:43], v[84:87]\n\t"
    "s_mov_b32 m0, s22\n\t"
    "v_mfma_f32_16x16x32_bf16 v[88:91], a[224:227], a[40:43], v[88:91]\n\t"
    "buffer_load_dwordx4 v4, s[16:19], s20 offen offset:0 sc1 lds\n\t"
    "v_mfma_f32_16x16x32_bf16 v[92:95], a[236:239], a[40:43], v[92:95]\n\t"
    "buffer_load_dwordx4 v4, s[16:19], s20 offen offset:1024 sc1 lds\n\t"
    "v_mfma_f32_16x16x32_bf16 v[72:75], a[180:183], a[44:47], v[72:75]\n\t"
    "buffer_load_dwordx4 v4, s[16:19], s20 offen offset:2048 sc1 lds\n\t"
    "s_add_u32 s20, s20, 12288\n\t"
    "v_mfma_f32_16x16x32_bf16 v[76:79], a[192:195], a[44:47], v[76:79]\n\t"
    "s_add_u32 s22, s22, 12288\n\t"
    "v_mfma_f32_16x16x32_bf16 v[80:83], a[204:207], a[44:47], v[80:83]\n\t"
    "s_mov_b32 m0, s22\n\t"
    "v_mfma_f32_16x16x32_bf16 v[84:87], a[216:219], a[44:47], v[84:87]\n\t"
    "buffer_load_dwordx4 v4, s[16:19], s20 offen offset:0 sc1 lds\n\t"
    "v_mfma_f32_16x16x32_bf16 v[88:91], a[228:231], a[44:47], v[88:91]\n\t"
    "buffer_load_dwordx4 v4, s[16:19], s20 offen offset:1024 sc1 lds\n\t"
    "v_cvt_pk_bf16_f32 v196, v72, v73\n\t"
    "v_cvt_pk_bf16_f32 v197, v74, v75\n\t"
    "v_mfma_f32_16x16x32_bf16 v[92:95], a[240:243], a[44:47], v[92:95]\n\t"
    "buffer_load_dwordx4 v4, s[16:19], s20 offen offset:2048 sc1 lds\n\t"
    "s_add_u32 s20, s20, 12288\n\t"
    "v_cvt_pk_bf16_f32 v198, v76, v77\n\t"
    "v_cvt_pk_bf16_f32 v199, v78, v79\n\t"
    "s_waitcnt lgkmcnt(0)\n\t"
    "v_mfma_f32_16x16x32_bf16 v[8:11], v[144:147], v[160:163], 0\n\t"
    "s_add_u32 s22, s22, 12288\n\t"
    "v_cvt_pk_bf16_f32 v200, v80, v81\n\t"
    "v_cvt_pk_bf16_f32 v201, v82, v83\n\t"
    "v_mfma_f32_16x16x32_bf16 v[8:11], v[148:151], v[164:167], v[8:11]\n\t"
    "s_mov_b32 m0, s22\n\t"
    "v_cvt_pk_bf16_f32 v202, v84, v85\n\t"
    "v_cvt_pk_bf16_f32 v203, v86, v87\n\t"
    "v_mfma_f32_16x16x32_bf16 v[8:11], v[152:155], v[168:171], v[8:11]\n\t"
    "buffer_load_dwordx4 v4, s[16:19], s20 offen offset:0 sc1 lds\n\t"
    "v_cvt_pk_bf16_f32 v204, v88, v89\n\t"
    "v_cvt_pk_bf16_f32 v205, v90, v91\n\t"
    "v_mfma_f32_16x16x32_bf16 v[12:15], v[144:147], v[172:175], 0\n\t"
    "buffer_load_dwordx4 v4, s[16:19], s20 offen offset:1024 sc1 lds\n\t"
    "v_cvt_pk_bf16_f32 v206, v92, v93\n\t"
    "v_cvt_pk_bf16_f32 v207, v94, v95\n\t"
    "v_mfma_f32_16x16x32_bf16 v[12:15], v[148:151], v[176:179], v[12:15]\n\t"
    "buffer_load_dwordx4 v4, s[16:19], s20 offen offset:2048 sc1 lds\n\t"
    "s_add_u32 s20, s20, 12288\n\t"
    "v_mfma_f32_16x16x32_bf16 v[12:15], v[152:155], v[180:183], v[12:15]\n\t"
    "s_add_u32 s22, s22, 12288\n\t"
    "v_mfma_f32_16x16x32_bf16 v[16:19], v[144:147], v[184:187], 0\n\t"
    "s_mov_b32 m0, s22\n\t"
    "v_mfma_f32_16x16x32_bf16 v[16:19], v[148:151], v[188:191], v[16:19]\n\t"
    "buffer_load_dwordx4 v4, s[16:19], s20 offen offset:0 sc1 lds\n\t"
    "v_mfma_f32_16x16x32_bf16 v[16:19], v[152:155], v[192:195], v[16:19]\n\t"
    "buffer_load_dwordx4 v4, s[16:19], s20 offen offset:1024 sc1 lds\n\t"
    "v_mfma_f32_16x16x32_bf16 v[20:23], v[144:147], v[196:199], 0\n\t"
    "buffer_load_dwordx4 v4, s[16:19], s20 offen offset:2048 sc1 lds\n\t"
    "s_add_u32 s20, s20, 12288\n\t"
    "v_mfma_f32_16x16x32_bf16 v[20:23], v[148:151], v[200:203], v[20:23]\n\t"
    "s_add_u32 s22, s22, 12288\n\t"
    "v_mfma_f32_16x16x32_bf16 v[20:23], v[152:155], v[204:207], v[20:23]\n\t"
    "v_max3_f32 v8, v8, v9, v10\n\t"
    "v_max3_f32 v12, v12, v13, v14\n\t"
    "v_max3_f32 v16, v16, v17, v18\n\t"
    "s_nop 4\n\t"
    "v_max3_f32 v20, v20, v21, v22\n\t"
    "v_max_f32 v8, v8, v11\n\t"
    "v_max_f32 v12, v12, v15\n\t"
    "v_max_f32 v16, v16, v19\n\t"
    "v_max_f32 v20, v20, v23\n\t"
    "v_xor_b32 v9, 0x80000000, v8\n\t"
    "v_xor_b32 v10, 0x80000000, v8\n\t"
    "v_xor_b32 v11, 0x80000000, v8\n\t"
    "v_xor_b32 v8, 0x80000000, v8\n\t"
    "v_xor_b32 v13, 0x80000000, v12\n\t"
    "v_xor_b32 v14, 0x80000000, v12\n\t"
    "v_xor_b32 v15, 0x80000000, v12\n\t"
    "v_xor_b32 v12, 0x80000000, v12\n\t"
    "v_xor_b32 v17, 0x80000000, v16\n\t"
    "v_xor_b32 v18, 0x80000000, v16\n\t"
    "v_xor_b32 v19, 0x80000000, v16\n\t"
    "v_xor_b32 v16, 0x80000000, v16\n\t"
    "v_xor_b32 v21, 0x80000000, v20\n\t"
    "v_xor_b32 v22, 0x80000000, v20\n\t"
    "v_xor_b32 v23, 0x80000000, v20\n\t"
    "v_xor_b32 v20, 0x80000000, v20\n\t"
    "s_waitcnt vmcnt(9) lgkmcnt(0)\n\t"
    "s_barrier\n\t"
    "ds_read_b128 a[0:3], v0 offset:0\n\t"
    "ds_read_b128 a[12:15], v0 offset:3072\n\t"
    "ds_read_b128 a[4:7], v0 offset:1024\n\t"
    "ds_read_b128 a[16:19], v0 offset:4096\n\t"
    "ds_read_b128 a[8:11], v0 offset:2048\n\t"
    "ds_read_b128 a[20:23], v0 offset:5120\n\t"
    "s_waitcnt lgkmcnt(0)\n\t"
    "v_mfma_f32_16x16x32_bf16 v[24:27], a[0:3], v[160:163], v[8:11]\n\t"
    "s_mov_b32 m0, s22\n\t"
    "ds_read2st64_b64 a[48:51], v1 offset0:12 offset1:13\n\t"
    "ds_read2st64_b64 a[52:55], v1 offset0:14 offset1:15\n\t"
    "v_mfma_f32_16x16x32_bf16 v[28:31], a[12:15], v[160:163], v[8:11]\n\t"
    "ds_read2st64_b64 a[56:59], v1 offset0:16 offset1:17\n\t"
    "ds_read2st64_b64 a[60:63], v1 offset0:18 offset1:19\n\t"
    "v_mfma_f32_16x16x32_bf16 v[24:27], a[4:7], v[164:167], v[24:27]\n\t"
    "ds_read2st64_b64 a[64:67], v1 offset0:20 offset1:21\n\t"
    "ds_read2st64_b64 a[68:71], v1 offset0:22 offset1:23\n\t"
    "v_mfma_f32_16x16x32_bf16 v[28:31], a[16:19], v[164:167], v[28:31]\n\t"
    "buffer_load_dwordx4 v4, s[16:19], s20 offen offset:0 sc1 lds\n\t"
    "buffer_load_dwordx4 v4, s[16:19], s20 offen offset:1024 sc1 lds\n\t"
    "v_mfma_f32_16x16x32_bf16 v[24:27], a[8:11], v[168:171], v[24:27]\n\t"
    "buffer_load_dwordx4 v4, s[16:19], s20 offen offset:2048 sc1 lds\n\t"
    "v_mfma_f32_16x16x32_bf16 v[28:31], a[20:23], v[168:171], v[28:31]\n\t"
    "s_add_u32 s20, s20, 12288\n\t"
    "s_add_u32 s22, s22, 12288\n\t"
    "v_mfma_f32_16x16x32_bf16 v[32:35], a[0:3], v[172:175], v[12:15]\n\t"
    "s_mov_b32 m0, s22\n\t"
    "s_nop 1\n\t"
    "v_exp_f32 v24, v24\n\t"
    "v_exp_f32 v25, v25\n\t"
    "v_mfma_f32_16x16x32_bf16 v[36:39], a[12:15], v[172:175], v[12:15]\n\t"
    "v_cvt_pk_bf16_f32 v40, v24, v25\n\t"
    "v_exp_f32 v26, v26\n\t"
    "v_mfma_f32_16x16x32_bf16 v[32:35], a[4:7], v[176:179], v[32:35]\n\t"
    "buffer_load_dwordx4 v4, s[16:19], s20 offen offset:0 sc1 lds\n\t"
    "buffer_load_dwordx4 v4, s[16:19], s20 offen offset:1024 sc1 lds\n\t"
    "v_exp_f32 v27, v27\n\t"
    "v_mfma_f32_16x16x32_bf16 v[36:39], a[16:19], v[176:179], v[36:39]\n\t"
    "v_cvt_pk_bf16_f32 v41, v26, v27\n\t"
    "v_exp_f32 v28, v28\n\t"
    "v_exp_f32 v29, v29\n\t"
    "v_mfma_f32_16x16x32_bf16 v[32:35], a[8:11], v[180:183], v[32:35]\n\t"
    "v_cvt_pk_bf16_f32 v42, v28, v29\n\t"
    "v_exp_f32 v30, v30\n\t"
    "v_mfma_f32_16x16x32_bf16 v[36:39], a[20:23], v[180:183], v[36:39]\n\t"
    "v_exp_f32 v31, v31\n\t"
    "s_nop 0\n\t"
    "v_cvt_pk_bf16_f32 v43, v30, v31\n\t"
    "s_waitcnt vmcnt(11)\n\t"
    "s_barrier\n\t"
    "v_mfma_f32_16x16x32_bf16 v[24:27], a[0:3], v[184:187], v[16:19]\n\t"
    "v_exp_f32 v32, v32\n\t"
    "v_mfma_f32_16x16x32_bf16 v[28:31], a[12:15], v[184:187], v[16:19]\n\t"
    "v_exp_f32 v33, v33\n\t"
    "v_mfma_f32_16x16x32_bf16 v[24:27], a[4:7], v[188:191], v[24:27]\n\t"
    "ds_read_b128 a[24:27], v0 offset:12288\n\t"
    "v_cvt_pk_bf16_f32 v44, v32, v33\n\t"
    "v_mfma_f32_16x16x32_bf16 v[28:31], a[16:19], v[188:191], v[28:31]\n\t"
    "v_exp_f32 v34, v34\n\t"
    "v_mfma_f32_16x16x32_bf16 v[24:27], a[8:11], v[192:195], v[24:27]\n\t"
    "v_exp_f32 v35, v35\n\t"
    "v_mfma_f32_16x16x32_bf16 v[28:31], a[20:23], v[192:195], v[28:31]\n\t"
    "s_waitcnt lgkmcnt(1)\n\t"
    "ds_read_b128 a[28:31], v0 offset:13312\n\t"
    "v_cvt_pk_bf16_f32 v45, v34, v35\n\t"
    "v_mfma_f32_16x16x32_bf16 v[48:51], a[48:51], v[40:43], 0\n\t"
    "v_exp_f32 v36, v36\n\t"
    "v_mfma_f32_16x16x32_bf16 v[52:55], a[52:55], v[40:43], 0\n\t"
    "v_exp_f32 v37, v37\n\t"
    "v_mfma_f32_16x16x32_bf16 v[56:59], a[56:59], v[40:43], 0\n\t"
    "ds_read_b128 a[32:35], v0 offset:14336\n\t"
    "v_cvt_pk_bf16_f32 v46, v36, v37\n\t"
    "v_mfma_f32_16x16x32_bf16 v[60:63], a[60:63], v[40:43], 0\n\t"
    "v_exp_f32 v38, v38\n\t"
    "v_mfma_f32_16x16x32_bf16 v[64:67], a[64:67], v[40:43], 0\n\t"
    "v_exp_f32 v39, v39\n\t"
    "v_mfma_f32_16x16x32_bf16 v[68:71], a[68:71], v[40:43], 0\n\t"
    "ds_read_b128 a[36:39], v0 offset:15360\n\t"
    "v_cvt_pk_bf16_f32 v47, v38, v39\n\t"
    "v_mfma_f32_16x16x32_bf16 v[72:75], a[96:99], v[40:43], 0\n\t"
    "ds_read_b128 a[40:43], v0 offset:16384\n\t"
    "ds_read_b128 a[44:47], v0 offset:17408\n\t"
    "v_mfma_f32_16x16x32_bf16 v[32:35], a[0:3], v[196:199], v[20:23]\n\t"
    "v_exp_f32 v24, v24\n\t"
    "v_mfma_f32_16x16x32_bf16 v[36:39], a[12:15], v[196:199], v[20:23]\n\t"
    "v_exp_f32 v25, v25\n\t"
    "v_mfma_f32_16x16x32_bf16 v[32:35], a[4:7], v[200:203], v[32:35]\n\t"
    "ds_read2st64_b64 a[72:75], v1 offset0:36 offset1:37\n\t"
    "v_cvt_pk_bf16_f32 v40, v24, v25\n\t"
    "v_mfma_f32_16x16x32_bf16 v[36:39], a[16:19], v[200:203], v[36:39]\n\t"
    "v_exp_f32 v26, v26\n\t"
    "v_mfma_f32_16x16x32_bf16 v[32:35], a[8:11], v[204:207], v[32:35]\n\t"
    "v_exp_f32 v27, v27\n\t"
    "v_mfma_f32_16x16x32_bf16 v[36:39], a[20:23], v[204:207], v[36:39]\n\t"
    "ds_read2st64_b64 a[76:79], v1 offset0:38 offset1:39\n\t"
    "v_cvt_pk_bf16_f32 v41, v26, v27\n\t"
    "v_mfma_f32_16x16x32_bf16 v[76:79], a[48:51], v[44:47], 0\n\t"
    "v_exp_f32 v28, v28\n\t"
    "v_mfma_f32_16x16x32_bf16 v[80:83], a[52:55], v[44:47], 0\n\t"
    "v_exp_f32 v29, v29\n\t"
    "v_mfma_f32_16x16x32_bf16 v[84:87], a[56:59], v[44:47], 0\n\t"
    "ds_read2st64_b64 a[80:83], v1 offset0:40 offset1:41\n\t"
    "v_cvt_pk_bf16_f32 v42, v28, v29\n\t"
    "v_mfma_f32_16x16x32_bf16 v[88:91], a[60:63], v[44:47], 0\n\t"
    "v_exp_f32 v30, v30\n\t"
    "v_mfma_f32_16x16x32_bf16 v[92:95], a[64:67], v[44:47], 0\n\t"
    "v_exp_f32 v31, v31\n\t"
    "v_mfma_f32_16x16x32_bf16 v[96:99], a[68:71], v[44:47], 0\n\t"
    "ds_read2st64_b64 a[84:87], v1 offset0:42 offset1:43\n\t"
    "v_cvt_pk_bf16_f32 v43, v30, v31\n\t"
    "v_mfma_f32_16x16x32_bf16 v[100:103], a[96:99], v[44:47], 0\n\t"
    "ds_read2st64_b64 a[88:91], v1 offset0:44 offset1:45\n\t"
    "ds_read2st64_b64 a[92:95], v1 offset0:46 offset1:47\n\t"
    "s_waitcnt vmcnt(5)\n\t"
    "s_barrier\n\t"
    "s_waitcnt lgkmcnt(6)\n\t"
    "v_mfma_f32_16x16x32_bf16 v[24:27], a[24:27], v[160:163], v[8:11]\n\t"
    "v_exp_f32 v32, v32\n\t"
    "v_mfma_f32_16x16x32_bf16 v[28:31], a[36:39], v[160:163], v[8:11]\n\t"
    "v_exp_f32 v33, v33\n\t"
    "v_mfma_f32_16x16x32_bf16 v[24:27], a[28:31], v[164:167], v[24:27]\n\t"
    "ds_read_b128 a[0:3], v0 offset:24576\n\t"
    "v_cvt_pk_bf16_f32 v44, v32, v33\n\t"
    "v_mfma_f32_16x16x32_bf16 v[28:31], a[40:43], v[164:167], v[28:31]\n\t"
    "v_exp_f32 v34, v34\n\t"
    "v_mfma_f32_16x16x32_bf16 v[24:27], a[32:35], v[168:171], v[24:27]\n\t"
    "v_exp_f32 v35, v35\n\t"
    "v_mfma_f32_16x16x32_bf16 v[28:31], a[44:47], v[168:171], v[28:31]\n\t"
    "ds_read_b128 a[4:7], v0 offset:25600\n\t"
    "v_cvt_pk_bf16_f32 v45, v34, v35\n\t"
    "v_mfma_f32_16x16x32_bf16 v[104:107], a[48:51], v[40:43], 0\n\t"
    "v_exp_f32 v36, v36\n\t"
    "v_mfma_f32_16x16x32_bf16 v[108:111], a[52:55], v[40:43], 0\n\t"
    "v_exp_f32 v37, v37\n\t"
    "v_mfma_f32_16x16x32_bf16 v[112:115], a[56:59], v[40:43], 0\n\t"
    "ds_read_b128 a[8:11], v0 offset:26624\n\t"
    "v_cvt_pk_bf16_f32 v46, v36, v37\n\t"
    "v_mfma_f32_16x16x32_bf16 v[116:119], a[60:63], v[40:43], 0\n\t"
    "v_exp_f32 v38, v38\n\t"
    "v_mfma_f32_16x16x32_bf16 v[120:123], a[64:67], v[40:43], 0\n\t"
    "v_exp_f32 v39, v39\n\t"
    "v_mfma_f32_16x16x32_bf16 v[124:127], a[68:71], v[40:43], 0\n\t"
    "buffer_load_dwordx4 v4, s[16:19], s20 offen offset:2048 sc1 lds\n\t"
    "v_cvt_pk_bf16_f32 v47, v38, v39\n\t"
    "v_mfma_f32_16x16x32_bf16 v[128:131], a[96:99], v[40:43], 0\n\t"
    "s_add_u32 s20, s20, 12288\n\t"
    "s_add_u32 s22, s22, 12288\n\t"
    "s_mov_b32 m0, s22\n\t"
    "s_nop 0\n\t"
    "buffer_load_dwordx4 v4, s[16:19], s20 offen offset:0 sc1 lds\n\t"
    "v_mfma_f32_16x16x32_bf16 v[32:35], a[24:27], v[172:175], v[12:15]\n\t"
    "v_exp_f32 v24, v24\n\t"
    "v_mfma_f32_16x16x32_bf16 v[36:39], a[36:39], v[172:175], v[12:15]\n\t"
    "v_exp_f32 v25, v25\n\t"
    "v_mfma_f32_16x16x32_bf16 v[32:35], a[28:31], v[176:179], v[32:35]\n\t"
    "ds_read_b128 a[12:15], v0 offset:27648\n\t"
    "v_cvt_pk_bf16_f32 v40, v24, v25\n\t"
    "v_mfma_f32_16x16x32_bf16 v[36:39], a[40:43], v[176:179], v[36:39]\n\t"
    "v_exp_f32 v26, v26\n\t"
    "v_mfma_f32_16x16x32_bf16 v[32:35], a[32:35], v[180:183], v[32:35]\n\t"
    "v_exp_f32 v27, v27\n\t"
    "v_mfma_f32_16x16x32_bf16 v[36:39], a[44:47], v[180:183], v[36:39]\n\t"
    "ds_read_b128 a[16:19], v0 offset:28672\n\t"
    "v_cvt_pk_bf16_f32 v41, v26, v27\n\t"
    "v_mfma_f32_16x16x32_bf16 v[132:135], a[48:51], v[44:47], 0\n\t"
    "v_exp_f32 v28, v28\n\t"
    "v_mfma_f32_16x16x32_bf16 v[136:139], a[52:55], v[44:47], 0\n\t"
    "v_exp_f32 v29, v29\n\t"
    "v_mfma_f32_16x16x32_bf16 v[140:143], a[56:59], v[44:47], 0\n\t"
    "ds_read_b128 a[20:23], v0 offset:29696\n\t"
    "v_cvt_pk_bf16_f32 v42, v28, v29\n\t"
    "v_mfma_f32_16x16x32_bf16 v[144:147], a[60:63], v[44:47], 0\n\t"
    "v_exp_f32 v30, v30\n\t"
    "v_mfma_f32_16x16x32_bf16 v[148:151], a[64:67], v[44:47], 0\n\t"
    "v_exp_f32 v31, v31\n\t"
    "v_mfma_f32_16x16x32_bf16 v[152:155], a[68:71], v[44:47], 0\n\t"
    "buffer_load_dwordx4 v4, s[16:19], s20 offen offset:1024 sc1 lds\n\t"
    "v_cvt_pk_bf16_f32 v43, v30, v31\n\t"
    "v_mfma_f32_16x16x32_bf16 v[156:159], a[96:99], v[44:47], 0\n\t"
    "buffer_load_dwordx4 v4, s[16:19], s20 offen offset:2048 sc1 lds\n\t"
    "s_add_u32 s20, s20, 12288\n\t"
    "s_add_u32 s22, s22, 12288\n\t"
    "v_mfma_f32_16x16x32_bf16 v[24:27], a[24:27], v[184:187], v[16:19]\n\t"
    "v_exp_f32 v32, v32\n\t"
    "v_mfma_f32_16x16x32_bf16 v[28:31], a[36:39], v[184:187], v[16:19]\n\t"
    "v_exp_f32 v33, v33\n\t"
    "v_mfma_f32_16x16x32_bf16 v[24:27], a[28:31], v[188:191], v[24:27]\n\t"
    "ds_read2st64_b64 a[48:51], v1 offset0:60 offset1:61\n\t"
    "v_cvt_pk_bf16_f32 v44, v32, v33\n\t"
    "v_mfma_f32_16x16x32_bf16 v[28:31], a[40:43], v[188:191], v[28:31]\n\t"
    "v_exp_f32 v34, v34\n\t"
    "v_mfma_f32_16x16x32_bf16 v[24:27], a[32:35], v[192:195], v[24:27]\n\t"
    "v_exp_f32 v35, v35\n\t"
    "v_mfma_f32_16x16x32_bf16 v[28:31], a[44:47], v[192:195], v[28:31]\n\t"
    "s_waitcnt lgkmcnt(7)\n\t"
    "ds_read2st64_b64 a[52:55], v1 offset0:62 offset1:63\n\t"
    "v_cvt_pk_bf16_f32 v45, v34, v35\n\t"
    "v_mfma_f32_16x16x32_bf16 v[48:51], a[72:75], v[40:43], v[48:51]\n\t"
    "v_exp_f32 v36, v36\n\t"
    "v_mfma_f32_16x16x32_bf16 v[52:55], a[76:79], v[40:43], v[52:55]\n\t"
    "v_exp_f32 v37, v37\n\t"
    "v_mfma_f32_16x16x32_bf16 v[56:59], a[80:83], v[40:43], v[56:59]\n\t"
    "ds_read2st64_b64 a[56:59], v1 offset0:64 offset1:65\n\t"
    "v_cvt_pk_bf16_f32 v46, v36, v37\n\t"
    "v_mfma_f32_16x16x32_bf16 v[60:63], a[84:87], v[40:43], v[60:63]\n\t"
    "v_exp_f32 v38, v38\n\t"
    "v_mfma_f32_16x16x32_bf16 v[64:67], a[88:91], v[40:43], v[64:67]\n\t"
    "v_exp_f32 v39, v39\n\t"
    "v_mfma_f32_16x16x32_bf16 v[68:71], a[92:95], v[40:43], v[68:71]\n\t"
    "ds_read2st64_b64 a[60:63], v1 offset0:66 offset1:67\n\t"
    "v_cvt_pk_bf16_f32 v47, v38, v39\n\t"
    "v_mfma_f32_16x16x32_bf16 v[72:75], a[96:99], v[40:43], v[72:75]\n\t"
    "ds_read2st64_b64 a[64:67], v1 offset0:68 offset1:69\n\t"
    "v_mfma_f32_16x16x32_bf16 v[32:35], a[24:27], v[196:199], v[20:23]\n\t"
    "s_mov_b32 m0, s22\n\t"
    "v_exp_f32 v24, v24\n\t"
    "v_mfma_f32_16x16x32_bf16 v[36:39], a[36:39], v[196:199], v[20:23]\n\t"
    "v_exp_f32 v25, v25\n\t"
    "v_mfma_f32_16x16x32_bf16 v[32:35], a[28:31], v[200:203], v[32:35]\n\t"
    "ds_read2st64_b64 a[68:71], v1 offset0:70 offset1:71\n\t"
    "v_cvt_pk_bf16_f32 v40, v24, v25\n\t"
    "v_mfma_f32_16x16x32_bf16 v[36:39], a[40:43], v[200:203], v[36:39]\n\t"
    "v_exp_f32 v26, v26\n\t"
    "v_mfma_f32_16x16x32_bf16 v[32:35], a[32:35], v[204:207], v[32:35]\n\t"
    "v_exp_f32 v27, v27\n\t"
    "v_mfma_f32_16x16x32_bf16 v[36:39], a[44:47], v[204:207], v[36:39]\n\t"
    "buffer_load_dwordx4 v4, s[16:19], s20 offen offset:0 sc1 lds\n\t"
    "v_cvt_pk_bf16_f32 v41, v26, v27\n\t"
    "v_mfma_f32_16x16x32_bf16 v[76:79], a[72:75], v[44:47], v[76:79]\n\t"
    "v_exp_f32 v28, v28\n\t"
    "v_mfma_f32_16x16x32_bf16 v[80:83], a[76:79], v[44:47], v[80:83]\n\t"
    "v_exp_f32 v29, v29\n\t"
    "v_mfma_f32_16x16x32_bf16 v[84:87], a[80:83], v[44:47], v[84:87]\n\t"
    "buffer_load_dwordx4 v4, s[16:19], s20 offen offset:1024 sc1 lds\n\t"
    "v_cvt_pk_bf16_f32 v42, v28, v29\n\t"
    "v_mfma_f32_16x16x32_bf16 v[88:91], a[84:87], v[44:47], v[88:91]\n\t"
    "v_exp_f32 v30, v30\n\t"
    "v_mfma_f32_16x16x32_bf16 v[92:95], a[88:91], v[44:47], v[92:95]\n\t"
    "v_exp_f32 v31, v31\n\t"
    "v_mfma_f32_16x16x32_bf16 v[96:99], a[92:95], v[44:47], v[96:99]\n\t"
    "buffer_load_dwordx4 v4, s[16:19], s20 offen offset:2048 sc1 lds\n\t"
    "v_cvt_pk_bf16_f32 v43, v30, v31\n\t"
    "v_mfma_f32_16x16x32_bf16 v[100:103], a[96:99], v[44:47], v[100:103]\n\t"
    "s_add_u32 s20, s20, 12288\n\t"
    "s_add_u32 s22, s22, 12288\n\t"
    "s_waitcnt lgkmcnt(6)\n\t"
    "v_mfma_f32_16x16x32_bf16 v[24:27], a[0:3], v[160:163], v[8:11]\n\t"
    "v_exp_f32 v32, v32\n\t"
    "v_mfma_f32_16x16x32_bf16 v[28:31], a[12:15], v[160:163], v[8:11]\n\t"
    "v_exp_f32 v33, v33\n\t"
    "v_mfma_f32_16x16x32_bf16 v[24:27], a[4:7], v[164:167], v[24:27]\n\t"
    "ds_read_b128 a[24:27], v0 offset:36864\n\t"
    "v_cvt_pk_bf16_f32 v44, v32, v33\n\t"
    "v_mfma_f32_16x16x32_bf16 v[28:31], a[16:19], v[164:167], v[28:31]\n\t"
    "v_exp_f32 v34, v34\n\t"
    "v_mfma_f32_16x16x32_bf16 v[24:27], a[8:11], v[168:171], v[24:27]\n\t"
    "v_exp_f32 v35, v35\n\t"
    "v_mfma_f32_16x16x32_bf16 v[28:31], a[20:23], v[168:171], v[28:31]\n\t"
    "ds_read_b128 a[28:31], v0 offset:37888\n\t"
    "v_cvt_pk_bf16_f32 v45, v34, v35\n\t"
    "v_mfma_f32_16x16x32_bf16 v[104:107], a[72:75], v[40:43], v[104:107]\n\t"
    "v_exp_f32 v36, v36\n\t"
    "v_mfma_f32_16x16x32_bf16 v[108:111], a[76:79], v[40:43], v[108:111]\n\t"
    "v_exp_f32 v37, v37\n\t"
    "v_mfma_f32_16x16x32_bf16 v[112:115], a[80:83], v[40:43], v[112:115]\n\t"
    "ds_read_b128 a[32:35], v0 offset:38912\n\t"
    "v_cvt_pk_bf16_f32 v46, v36, v37\n\t"
    "v_mfma_f32_16x16x32_bf16 v[116:119], a[84:87], v[40:43], v[116:119]\n\t"
    "v_exp_f32 v38, v38\n\t"
    "v_mfma_f32_16x16x32_bf16 v[120:123], a[88:91], v[40:43], v[120:123]\n\t"
    "v_exp_f32 v39, v39\n\t"
    "v_mfma_f32_16x16x32_bf16 v[124:127], a[92:95], v[40:43], v[124:127]\n\t"
    "v_cvt_pk_bf16_f32 v47, v38, v39\n\t"
    "v_mfma_f32_16x16x32_bf16 v[128:131], a[96:99], v[40:43], v[128:131]\n\t"
    "v_mfma_f32_16x16x32_bf16 v[32:35], a[0:3], v[172:175], v[12:15]\n\t"
    "v_exp_f32 v24, v24\n\t"
    "v_mfma_f32_16x16x32_bf16 v[36:39], a[12:15], v[172:175], v[12:15]\n\t"
    "v_exp_f32 v25, v25\n\t"
    "v_mfma_f32_16x16x32_bf16 v[32:35], a[4:7], v[176:179], v[32:35]\n\t"
    "ds_read_b128 a[36:39], v0 offset:39936\n\t"
    "v_cvt_pk_bf16_f32 v40, v24, v25\n\t"
    "v_mfma_f32_16x16x32_bf16 v[36:39], a[16:19], v[176:179], v[36:39]\n\t"
    "v_exp_f32 v26, v26\n\t"
    "v_mfma_f32_16x16x32_bf16 v[32:35], a[8:11], v[180:183], v[32:35]\n\t"
    "v_exp_f32 v27, v27\n\t"
    "v_mfma_f32_16x16x32_bf16 v[36:39], a[20:23], v[180:183], v[36:39]\n\t"
    "ds_read_b128 a[40:43], v0 offset:40960\n\t"
    "v_cvt_pk_bf16_f32 v41, v26, v27\n\t"
    "v_mfma_f32_16x16x32_bf16 v[132:135], a[72:75], v[44:47], v[132:135]\n\t"
    "v_exp_f32 v28, v28\n\t"
    "v_mfma_f32_16x16x32_bf16 v[136:139], a[76:79], v[44:47], v[136:139]\n\t"
    "v_exp_f32 v29, v29\n\t"
    "v_mfma_f32_16x16x32_bf16 v[140:143], a[80:83], v[44:47], v[140:143]\n\t"
    "ds_read_b128 a[44:47], v0 offset:41984\n\t"
    "v_cvt_pk_bf16_f32 v42, v28, v29\n\t"
    "v_mfma_f32_16x16x32_bf16 v[144:147], a[84:87], v[44:47], v[144:147]\n\t"
    "v_exp_f32 v30, v30\n\t"
    "v_mfma_f32_16x16x32_bf16 v[148:151], a[88:91], v[44:47], v[148:151]\n\t"
    "v_exp_f32 v31, v31\n\t"
    "v_mfma_f32_16x16x32_bf16 v[152:155], a[92:95], v[44:47], v[152:155]\n\t"
    "v_cvt_pk_bf16_f32 v43, v30, v31\n\t"
    "v_mfma_f32_16x16x32_bf16 v[156:159], a[96:99], v[44:47], v[156:159]\n\t"
    "v_mfma_f32_16x16x32_bf16 v[24:27], a[0:3], v[184:187], v[16:19]\n\t"
    "v_exp_f32 v32, v32\n\t"
    "v_mfma_f32_16x16x32_bf16 v[28:31], a[12:15], v[184:187], v[16:19]\n\t"
    "v_exp_f32 v33, v33\n\t"
    "v_mfma_f32_16x16x32_bf16 v[24:27], a[4:7], v[188:191], v[24:27]\n\t"
    "ds_read2st64_b64 a[72:75], v1 offset0:84 offset1:85\n\t"
    "v_cvt_pk_bf16_f32 v44, v32, v33\n\t"
    "v_mfma_f32_16x16x32_bf16 v[28:31], a[16:19], v[188:191], v[28:31]\n\t"
    "v_exp_f32 v34, v34\n\t"
    "v_mfma_f32_16x16x32_bf16 v[24:27], a[8:11], v[192:195], v[24:27]\n\t"
    "v_exp_f32 v35, v35\n\t"
    "v_mfma_f32_16x16x32_bf16 v[28:31], a[20:23], v[192:195], v[28:31]\n\t"
    "s_waitcnt lgkmcnt(7)\n\t"
    "ds_read2st64_b64 a[76:79], v1 offset0:86 offset1:87\n\t"
    "v_cvt_pk_bf16_f32 v45, v34, v35\n\t"
    "v_mfma_f32_16x16x32_bf16 v[48:51], a[48:51], v[40:43], v[48:51]\n\t"
    "v_exp_f32 v36, v36\n\t"
    "v_mfma_f32_16x16x32_bf16 v[52:55], a[52:55], v[40:43], v[52:55]\n\t"
    "v_exp_f32 v37, v37\n\t"
    "v_mfma_f32_16x16x32_bf16 v[56:59], a[56:59], v[40:43], v[56:59]\n\t"
    "ds_read2st64_b64 a[80:83], v1 offset0:88 offset1:89\n\t"
    "v_cvt_pk_bf16_f32 v46, v36, v37\n\t"
    "v_mfma_f32_16x16x32_bf16 v[60:63], a[60:63], v[40:43], v[60:63]\n\t"
    "v_exp_f32 v38, v38\n\t"
    "v_mfma_f32_16x16x32_bf16 v[64:67], a[64:67], v[40:43], v[64:67]\n\t"
    "v_exp_f32 v39, v39\n\t"
    "v_mfma_f32_16x16x32_bf16 v[68:71], a[68:71], v[40:43], v[68:71]\n\t"
    "ds_read2st64_b64 a[84:87], v1 offset0:90 offset1:91\n\t"
    "v_cvt_pk_bf16_f32 v47, v38, v39\n\t"
    "v_mfma_f32_16x16x32_bf16 v[72:75], a[96:99], v[40:43], v[72:75]\n\t"
    "ds_read2st64_b64 a[88:91], v1 offset0:92 offset1:93\n\t"
    "v_mfma_f32_16x16x32_bf16 v[32:35], a[0:3], v[196:199], v[20:23]\n\t"
    "s_xor_b32 s23, s23, 49152\n\t"
    "v_exp_f32 v24, v24\n\t"
    "v_mfma_f32_16x16x32_bf16 v[36:39], a[12:15], v[196:199], v[20:23]\n\t"
    "s_add_u32 s22, s23, s25\n\t"
    "v_exp_f32 v25, v25\n\t"
    "v_mfma_f32_16x16x32_bf16 v[32:35], a[4:7], v[200:203], v[32:35]\n\t"
    "s_sub_u32 s24, s24, 1\n\t"
    "ds_read2st64_b64 a[92:95], v1 offset0:94 offset1:95\n\t"
    "v_cvt_pk_bf16_f32 v40, v24, v25\n\t"
    "v_mfma_f32_16x16x32_bf16 v[36:39], a[16:19], v[200:203], v[36:39]\n\t"
    "v_exp_f32 v26, v26\n\t"
    "v_mfma_f32_16x16x32_bf16 v[32:35], a[8:11], v[204:207], v[32:35]\n\t"
    "v_exp_f32 v27, v27\n\t"
    "v_mfma_f32_16x16x32_bf16 v[36:39], a[20:23], v[204:207], v[36:39]\n\t"
    "v_cvt_pk_bf16_f32 v41, v26, v27\n\t"
    "v_xor_b32 v1, 0xc000, v1\n\t"
    "v_mfma_f32_16x16x32_bf16 v[76:79], a[48:51], v[44:47], v[76:79]\n\t"
    "v_exp_f32 v28, v28\n\t"
    "v_mfma_f32_16x16x32_bf16 v[80:83], a[52:55], v[44:47], v[80:83]\n\t"
    "v_exp_f32 v29, v29\n\t"
    "v_mfma_f32_16x16x32_bf16 v[84:87], a[56:59], v[44:47], v[84:87]\n\t"
    "v_cvt_pk_bf16_f32 v42, v28, v29\n\t"
    "v_xor_b32 v0, 0xc000, v0\n\t"
    "v_mfma_f32_16x16x32_bf16 v[88:91], a[60:63], v[44:47], v[88:91]\n\t"
    "v_exp_f32 v30, v30\n\t"
    "v_mfma_f32_16x16x32_bf16 v[92:95], a[64:67], v[44:47], v[92:95]\n\t"
    "v_exp_f32 v31, v31\n\t"
    "v_mfma_f32_16x16x32_bf16 v[96:99], a[68:71], v[44:47], v[96:99]\n\t"
    "v_cvt_pk_bf16_f32 v43, v30, v31\n\t"
    "v_mfma_f32_16x16x32_bf16 v[100:103], a[96:99], v[44:47], v[100:103]\n\t"
    "1:\n\t"
    "s_waitcnt vmcnt(0) lgkmcnt(0)\n\t"
    "s_barrier\n\t"
    "s_waitcnt lgkmcnt(6)\n\t"
    "v_mfma_f32_16x16x32_bf16 v[24:27], a[24:27], v[160:163], v[8:11]\n\t"
    "s_mov_b32 m0, s22\n\t"
    "v_exp_f32 v32, v32\n\t"
    "v_mfma_f32_16x16x32_bf16 v[28:31], a[36:39], v[160:163], v[8:11]\n\t"
    "v_exp_f32 v33, v33\n\t"
    "v_mfma_f32_16x16x32_bf16 v[24:27], a[28:31], v[164:167], v[24:27]\n\t"
    "ds_read_b128 a[0:3], v0 offset:0\n\t"
    "v_cvt_pk_bf16_f32 v44, v32, v33\n\t"
    "v_mfma_f32_16x16x32_bf16 v[28:31], a[40:43], v[164:167], v[28:31]\n\t"
    "v_exp_f32 v34, v34\n\t"
    "v_mfma_f32_16x16x32_bf16 v[24:27], a[32:35], v[168:171], v[24:27]\n\t"
    "v_exp_f32 v35, v35\n\t"
    "v_mfma_f32_16x16x32_bf16 v[28:31], a[44:47], v[168:171], v[28:31]\n\t"
    "ds_read_b128 a[4:7], v0 offset:1024\n\t"
    "v_cvt_pk_bf16_f32 v45, v34, v35\n\t"
    "v_mfma_f32_16x16x32_bf16 v[104:107], a[48:51], v[40:43], v[104:107]\n\t"
    "v_exp_f32 v36, v36\n\t"
    "v_mfma_f32_16x16x32_bf16 v[108:111], a[52:55], v[40:43], v[108:111]\n\t"
    "v_exp_f32 v37, v37\n\t"
    "v_mfma_f32_16x16x32_bf16 v[112:115], a[56:59], v[40:43], v[112:115]\n\t"
    "ds_read_b128 a[8:11], v0 offset:2048\n\t"
    "v_cvt_pk_bf16_f32 v46, v36, v37\n\t"
    "v_mfma_f32_16x16x32_bf16 v[116:119], a[60:63], v[40:43], v[116:119]\n\t"
    "v_exp_f32 v38, v38\n\t"
    "v_mfma_f32_16x16x32_bf16 v[120:123], a[64:67], v[40:43], v[120:123]\n\t"
    "v_exp_f32 v39, v39\n\t"
    "v_mfma_f32_16x16x32_bf16 v[124:127], a[68:71], v[40:43], v[124:127]\n\t"
    "buffer_load_dwordx4 v4, s[16:19], s20 offen offset:0 sc1 lds\n\t"
    "v_cvt_pk_bf16_f32 v47, v38, v39\n\t"
    "v_mfma_f32_16x16x32_bf16 v[128:131], a[96:99], v[40:43], v[128:131]\n\t"
    "v_mfma_f32_16x16x32_bf16 v[32:35], a[24:27], v[172:175], v[12:15]\n\t"
    "v_exp_f32 v24, v24\n\t"
    "v_mfma_f32_16x16x32_bf16 v[36:39], a[36:39], v[172:175], v[12:15]\n\t"
    "v_exp_f32 v25, v25\n\t"
    "v_mfma_f32_16x16x32_bf16 v[32:35], a[28:31], v[176:179], v[32:35]\n\t"
    "ds_read_b128 a[12:15], v0 offset:3072\n\t"
    "v_cvt_pk_bf16_f32 v40, v24, v25\n\t"
    "v_mfma_f32_16x16x32_bf16 v[36:39], a[40:43], v[176:179], v[36:39]\n\t"
    "v_exp_f32 v26, v26\n\t"
    "v_mfma_f32_16x16x32_bf16 v[32:35], a[32:35], v[180:183], v[32:35]\n\t"
    "v_exp_f32 v27, v27\n\t"
    "v_mfma_f32_16x16x32_bf16 v[36:39], a[44:47], v[180:183], v[36:39]\n\t"
    "ds_read_b128 a[16:19], v0 offset:4096\n\t"
    "v_cvt_pk_bf16_f32 v41, v26, v27\n\t"
    "v_mfma_f32_16x16x32_bf16 v[132:135], a[48:51], v[44:47], v[132:135]\n\t"
    "v_exp_f32 v28, v28\n\t"
    "v_mfma_f32_16x16x32_bf16 v[136:139], a[52:55], v[44:47], v[136:139]\n\t"
    "v_exp_f32 v29, v29\n\t"
    "v_mfma_f32_16x16x32_bf16 v[140:143], a[56:59], v[44:47], v[140:143]\n\t"
    "ds_read_b128 a[20:23], v0 offset:5120\n\t"
    "v_cvt_pk_bf16_f32 v42, v28, v29\n\t"
    "v_mfma_f32_16x16x32_bf16 v[144:147], a[60:63], v[44:47], v[144:147]\n\t"
    "v_exp_f32 v30, v30\n\t"
    "v_mfma_f32_16x16x32_bf16 v[148:151], a[64:67], v[44:47], v[148:151]\n\t"
    "v_exp_f32 v31, v31\n\t"
    "v_mfma_f32_16x16x32_bf16 v[152:155], a[68:71], v[44:47], v[152:155]\n\t"
    "buffer_load_dwordx4 v4, s[16:19], s20 offen offset:1024 sc1 lds\n\t"
    "v_cvt_pk_bf16_f32 v43, v30, v31\n\t"
    "v_mfma_f32_16x16x32_bf16 v[156:159], a[96:99], v[44:47], v[156:159]\n\t"
    "v_mfma_f32_16x16x32_bf16 v[24:27], a[24:27], v[184:187], v[16:19]\n\t"
    "v_exp_f32 v32, v32\n\t"
    "v_mfma_f32_16x16x32_bf16 v[28:31], a[36:39], v[184:187], v[16:19]\n\t"
    "v_exp_f32 v33, v33\n\t"
    "v_mfma_f32_16x16x32_bf16 v[24:27], a[28:31], v[188:191], v[24:27]\n\t"
    "ds_read2st64_b64 a[48:51], v1 offset0:12 offset1:13\n\t"
    "v_cvt_pk_bf16_f32 v44, v32, v33\n\t"
    "v_mfma_f32_16x16x32_bf16 v[28:31], a[40:43], v[188:191], v[28:31]\n\t"
    "v_exp_f32 v34, v34\n\t"
    "v_mfma_f32_16x16x32_bf16 v[24:27], a[32:35], v[192:195], v[24:27]\n\t"
    "v_exp_f32 v35, v35\n\t"
    "v_mfma_f32_16x16x32_bf16 v[28:31], a[44:47], v[192:195], v[28:31]\n\t"
    "s_waitcnt lgkmcnt(7)\n\t"
    "ds_read2st64_b64 a[52:55], v1 offset0:14 offset1:15\n\t"
    "v_cvt_pk_bf16_f32 v45, v34, v35\n\t"
    "v_mfma_f32_16x16x32_bf16 v[48:51], a[72:75], v[40:43], v[48:51]\n\t"
    "v_exp_f32 v36, v36\n\t"
    "v_mfma_f32_16x16x32_bf16 v[52:55], a[76:79], v[40:43], v[52:55]\n\t"
    "v_exp_f32 v37, v37\n\t"
    "v_mfma_f32_16x16x32_bf16 v[56:59], a[80:83], v[40:43], v[56:59]\n\t"
    "ds_read2st64_b64 a[56:59], v1 offset0:16 offset1:17\n\t"
    "v_cvt_pk_bf16_f32 v46, v36, v37\n\t"
    "v_mfma_f32_16x16x32_bf16 v[60:63], a[84:87], v[40:43], v[60:63]\n\t"
    "v_exp_f32 v38, v38\n\t"
    "v_mfma_f32_16x16x32_bf16 v[64:67], a[88:91], v[40:43], v[64:67]\n\t"
    "v_exp_f32 v39, v39\n\t"
    "v_mfma_f32_16x16x32_bf16 v[68:71], a[92:95], v[40:43], v[68:71]\n\t"
    "ds_read2st64_b64 a[60:63], v1 offset0:18 offset1:19\n\t"
    "v_cvt_pk_bf16_f32 v47, v38, v39\n\t"
    "v_mfma_f32_16x16x32_bf16 v[72:75], a[96:99], v[40:43], v[72:75]\n\t"
    "ds_read2st64_b64 a[64:67], v1 offset0:20 offset1:21\n\t"
    "buffer_load_dwordx4 v4, s[16:19], s20 offen offset:2048 sc1 lds\n\t"
    "s_add_u32 s20, s20, 12288\n\t"
    "s_add_u32 s22, s22, 12288\n\t"
    "v_mfma_f32_16x16x32_bf16 v[32:35], a[24:27], v[196:199], v[20:23]\n\t"
    "s_mov_b32 m0, s22\n\t"
    "v_exp_f32 v24, v24\n\t"
    "v_mfma_f32_16x16x32_bf16 v[36:39], a[36:39], v[196:199], v[20:23]\n\t"
    "v_exp_f32 v25, v25\n\t"
    "v_mfma_f32_16x16x32_bf16 v[32:35], a[28:31], v[200:203], v[32:35]\n\t"
    "ds_read2st64_b64 a[68:71], v1 offset0:22 offset1:23\n\t"
    "v_cvt_pk_bf16_f32 v40, v24, v25\n\t"
    "v_mfma_f32_16x16x32_bf16 v[36:39], a[40:43], v[200:203], v[36:39]\n\t"
    "v_exp_f32 v26, v26\n\t"
    "v_mfma_f32_16x16x32_bf16 v[32:35], a[32:35], v[204:207], v[32:35]\n\t"
    "v_exp_f32 v27, v27\n\t"
    "v_mfma_f32_16x16x32_bf16 v[36:39], a[44:47], v[204:207], v[36:39]\n\t"
    "buffer_load_dwordx4 v4, s[16:19], s20 offen offset:0 sc1 lds\n\t"
    "v_cvt_pk_bf16_f32 v41, v26, v27\n\t"
    "v_mfma_f32_16x16x32_bf16 v[76:79], a[72:75], v[44:47], v[76:79]\n\t"
    "v_exp_f32 v28, v28\n\t"
    "v_mfma_f32_16x16x32_bf16 v[80:83], a[76:79], v[44:47], v[80:83]\n\t"
    "v_exp_f32 v29, v29\n\t"
    "v_mfma_f32_16x16x32_bf16 v[84:87], a[80:83], v[44:47], v[84:87]\n\t"
    "v_cvt_pk_bf16_f32 v42, v28, v29\n\t"
    "v_mfma_f32_16x16x32_bf16 v[88:91], a[84:87], v[44:47], v[88:91]\n\t"
    "v_exp_f32 v30, v30\n\t"
    "v_mfma_f32_16x16x32_bf16 v[92:95], a[88:91], v[44:47], v[92:95]\n\t"
    "v_exp_f32 v31, v31\n\t"
    "v_mfma_f32_16x16x32_bf16 v[96:99], a[92:95], v[44:47], v[96:99]\n\t"
    "v_cvt_pk_bf16_f32 v43, v30, v31\n\t"
    "v_mfma_f32_16x16x32_bf16 v[100:103], a[96:99], v[44:47], v[100:103]\n\t"
    "s_waitcnt lgkmcnt(6)\n\t"
    "v_mfma_f32_16x16x32_bf16 v[24:27], a[0:3], v[160:163], v[8:11]\n\t"
    "v_exp_f32 v32, v32\n\t"
    "v_mfma_f32_16x16x32_bf16 v[28:31], a[12:15], v[160:163], v[8:11]\n\t"
    "v_exp_f32 v33, v33\n\t"
    "v_mfma_f32_16x16x32_bf16 v[24:27], a[4:7], v[164:167], v[24:27]\n\t"
    "ds_read_b128 a[24:27], v0 offset:12288\n\t"
    "v_cvt_pk_bf16_f32 v44, v32, v33\n\t"
    "v_mfma_f32_16x16x32_bf16 v[28:31], a[16:19], v[164:167], v[28:31]\n\t"
    "v_exp_f32 v34, v34\n\t"
    "v_mfma_f32_16x16x32_bf16 v[24:27], a[8:11], v[168:171], v[24:27]\n\t"
    "v_exp_f32 v35, v35\n\t"
    "v_mfma_f32_16x16x32_bf16 v[28:31], a[20:23], v[168:171], v[28:31]\n\t"
    "ds_read_b128 a[28:31], v0 offset:13312\n\t"
    "v_cvt_pk_bf16_f32 v45, v34, v35\n\t"
    "v_mfma_f32_16x16x32_bf16 v[104:107], a[72:75], v[40:43], v[104:107]\n\t"
    "v_exp_f32 v36, v36\n\t"
    "v_mfma_f32_16x16x32_bf16 v[108:111], a[76:79], v[40:43], v[108:111]\n\t"
    "v_exp_f32 v37, v37\n\t"
    "v_mfma_f32_16x16x32_bf16 v[112:115], a[80:83], v[40:43], v[112:115]\n\t"
    "ds_read_b128 a[32:35], v0 offset:14336\n\t"
    "v_cvt_pk_bf16_f32 v46, v36, v37\n\t"
    "v_mfma_f32_16x16x32_bf16 v[116:119], a[84:87], v[40:43], v[116:119]\n\t"
    "v_exp_f32 v38, v38\n\t"
    "v_mfma_f32_16x16x32_bf16 v[120:123], a[88:91], v[40:43], v[120:123]\n\t"
    "v_exp_f32 v39, v39\n\t"
    "v_mfma_f32_16x16x32_bf16 v[124:127], a[92:95], v[40:43], v[124:127]\n\t"
    "buffer_load_dwordx4 v4, s[16:19], s20 offen offset:1024 sc1 lds\n\t"
    "v_cvt_pk_bf16_f32 v47, v38, v39\n\t"
    "v_mfma_f32_16x16x32_bf16 v[128:131], a[96:99], v[40:43], v[128:131]\n\t"
    "v_mfma_f32_16x16x32_bf16 v[32:35], a[0:3], v[172:175], v[12:15]\n\t"
    "v_exp_f32 v24, v24\n\t"
    "v_mfma_f32_16x16x32_bf16 v[36:39], a[12:15], v[172:175], v[12:15]\n\t"
    "v_exp_f32 v25, v25\n\t"
    "v_mfma_f32_16x16x32_bf16 v[32:35], a[4:7], v[176:179], v[32:35]\n\t"
    "ds_read_b128 a[36:39], v0 offset:15360\n\t"
    "v_cvt_pk_bf16_f32 v40, v24, v25\n\t"
    "v_mfma_f32_16x16x32_bf16 v[36:39], a[16:19], v[176:179], v[36:39]\n\t"
    "v_exp_f32 v26, v26\n\t"
    "v_mfma_f32_16x16x32_bf16 v[32:35], a[8:11], v[180:183], v[32:35]\n\t"
    "v_exp_f32 v27, v27\n\t"
    "v_mfma_f32_16x16x32_bf16 v[36:39], a[20:23], v[180:183], v[36:39]\n\t"
    "ds_read_b128 a[40:43], v0 offset:16384\n\t"
    "v_cvt_pk_bf16_f32 v41, v26, v27\n\t"
    "v_mfma_f32_16x16x32_bf16 v[132:135], a[72:75], v[44:47], v[132:135]\n\t"
    "v_exp_f32 v28, v28\n\t"
    "v_mfma_f32_16x16x32_bf16 v[136:139], a[76:79], v[44:47], v[136:139]\n\t"
    "v_exp_f32 v29, v29\n\t"
    "v_mfma_f32_16x16x32_bf16 v[140:143], a[80:83], v[44:47], v[140:143]\n\t"
    "ds_read_b128 a[44:47], v0 offset:17408\n\t"
    "v_cvt_pk_bf16_f32 v42, v28, v29\n\t"
    "v_mfma_f32_16x16x32_bf16 v[144:147], a[84:87], v[44:47], v[144:147]\n\t"
    "v_exp_f32 v30, v30\n\t"
    "v_mfma_f32_16x16x32_bf16 v[148:151], a[88:91], v[44:47], v[148:151]\n\t"
    "v_exp_f32 v31, v31\n\t"
    "v_mfma_f32_16x16x32_bf16 v[152:155], a[92:95], v[44:47], v[152:155]\n\t"
    "buffer_load_dwordx4 v4, s[16:19], s20 offen offset:2048 sc1 lds\n\t"
    "v_cvt_pk_bf16_f32 v43, v30, v31\n\t"
    "v_mfma_f32_16x16x32_bf16 v[156:159], a[96:99], v[44:47], v[156:159]\n\t"
    "s_add_u32 s20, s20, 12288\n\t"
    "s_add_u32 s22, s22, 12288\n\t"
    "v_mfma_f32_16x16x32_bf16 v[24:27], a[0:3], v[184:187], v[16:19]\n\t"
    "s_mov_b32 m0, s22\n\t"
    "v_exp_f32 v32, v32\n\t"
    "v_mfma_f32_16x16x32_bf16 v[28:31], a[12:15], v[184:187], v[16:19]\n\t"
    "v_exp_f32 v33, v33\n\t"
    "v_mfma_f32_16x16x32_bf16 v[24:27], a[4:7], v[188:191], v[24:27]\n\t"
    "ds_read2st64_b64 a[72:75], v1 offset0:36 offset1:37\n\t"
    "v_cvt_pk_bf16_f32 v44, v32, v33\n\t"
    "v_mfma_f32_16x16x32_bf16 v[28:31], a[16:19], v[188:191], v[28:31]\n\t"
    "v_exp_f32 v34, v34\n\t"
    "v_mfma_f32_16x16x32_bf16 v[24:27], a[8:11], v[192:195], v[24:27]\n\t"
    "v_exp_f32 v35, v35\n\t"
    "v_mfma_f32_16x16x32_bf16 v[28:31], a[20:23], v[192:195], v[28:31]\n\t"
    "s_waitcnt lgkmcnt(7)\n\t"
    "ds_read2st64_b64 a[76:79], v1 offset0:38 offset1:39\n\t"
    "v_cvt_pk_bf16_f32 v45, v34, v35\n\t"
    "v_mfma_f32_16x16x32_bf16 v[48:51], a[48:51], v[40:43], v[48:51]\n\t"
    "v_exp_f32 v36, v36\n\t"
    "v_mfma_f32_16x16x32_bf16 v[52:55], a[52:55], v[40:43], v[52:55]\n\t"
    "v_exp_f32 v37, v37\n\t"
    "v_mfma_f32_16x16x32_bf16 v[56:59], a[56:59], v[40:43], v[56:59]\n\t"
    "ds_read2st64_b64 a[80:83], v1 offset0:40 offset1:41\n\t"
    "v_cvt_pk_bf16_f32 v46, v36, v37\n\t"
    "v_mfma_f32_16x16x32_bf16 v[60:63], a[60:63], v[40:43], v[60:63]\n\t"
    "v_exp_f32 v38, v38\n\t"
    "v_mfma_f32_16x16x32_bf16 v[64:67], a[64:67], v[40:43], v[64:67]\n\t"
    "v_exp_f32 v39, v39\n\t"
    "v_mfma_f32_16x16x32_bf16 v[68:71], a[68:71], v[40:43], v[68:71]\n\t"
    "ds_read2st64_b64 a[84:87], v1 offset0:42 offset1:43\n\t"
    "v_cvt_pk_bf16_f32 v47, v38, v39\n\t"
    "v_mfma_f32_16x16x32_bf16 v[72:75], a[96:99], v[40:43], v[72:75]\n\t"
    "ds_read2st64_b64 a[88:91], v1 offset0:44 offset1:45\n\t"
    "buffer_load_dwordx4 v4, s[16:19], s20 offen offset:0 sc1 lds\n\t"
    "v_mfma_f32_16x16x32_bf16 v[32:35], a[0:3], v[196:199], v[20:23]\n\t"
    "v_exp_f32 v24, v24\n\t"
    "v_mfma_f32_16x16x32_bf16 v[36:39], a[12:15], v[196:199], v[20:23]\n\t"
    "v_exp_f32 v25, v25\n\t"
    "v_mfma_f32_16x16x32_bf16 v[32:35], a[4:7], v[200:203], v[32:35]\n\t"
    "ds_read2st64_b64 a[92:95], v1 offset0:46 offset1:47\n\t"
    "v_cvt_pk_bf16_f32 v40, v24, v25\n\t"
    "v_mfma_f32_16x16x32_bf16 v[36:39], a[16:19], v[200:203], v[36:39]\n\t"
    "v_exp_f32 v26, v26\n\t"
    "v_mfma_f32_16x16x32_bf16 v[32:35], a[8:11], v[204:207], v[32:35]\n\t"
    "v_exp_f32 v27, v27\n\t"
    "v_mfma_f32_16x16x32_bf16 v[36:39], a[20:23], v[204:207], v[36:39]\n\t"
    "buffer_load_dwordx4 v4, s[16:19], s20 offen offset:1024 sc1 lds\n\t"
    "v_cvt_pk_bf16_f32 v41, v26, v27\n\t"
    "v_mfma_f32_16x16x32_bf16 v[76:79], a[48:51], v[44:47], v[76:79]\n\t"
    "v_exp_f32 v28, v28\n\t"
    "v_mfma_f32_16x16x32_bf16 v[80:83], a[52:55], v[44:47], v[80:83]\n\t"
    "v_exp_f32 v29, v29\n\t"
    "v_mfma_f32_16x16x32_bf16 v[84:87], a[56:59], v[44:47], v[84:87]\n\t"
    "v_cvt_pk_bf16_f32 v42, v28, v29\n\t"
    "v_mfma_f32_16x16x32_bf16 v[88:91], a[60:63], v[44:47], v[88:91]\n\t"
    "v_exp_f32 v30, v30\n\t"
    "v_mfma_f32_16x16x32_bf16 v[92:95], a[64:67], v[44:47], v[92:95]\n\t"
    "v_exp_f32 v31, v31\n\t"
    "v_mfma_f32_16x16x32_bf16 v[96:99], a[68:71], v[44:47], v[96:99]\n\t"
    "v_cvt_pk_bf16_f32 v43, v30, v31\n\t"
    "v_mfma_f32_16x16x32_bf16 v[100:103], a[96:99], v[44:47], v[100:103]\n\t"
    "s_waitcnt lgkmcnt(6)\n\t"
    "v_mfma_f32_16x16x32_bf16 v[24:27], a[24:27], v[160:163], v[8:11]\n\t"
    "v_exp_f32 v32, v32\n\t"
    "v_mfma_f32_16x16x32_bf16 v[28:31], a[36:39], v[160:163], v[8:11]\n\t"
    "v_exp_f32 v33, v33\n\t"
    "v_mfma_f32_16x16x32_bf16 v[24:27], a[28:31], v[164:167], v[24:27]\n\t"
    "ds_read_b128 a[0:3], v0 offset:24576\n\t"
    "v_cvt_pk_bf16_f32 v44, v32, v33\n\t"
    "v_mfma_f32_16x16x32_bf16 v[28:31], a[40:43], v[164:167], v[28:31]\n\t"
    "v_exp_f32 v34, v34\n\t"
    "v_mfma_f32_16x16x32_bf16 v[24:27], a[32:35], v[168:171], v[24:27]\n\t"
    "v_exp_f32 v35, v35\n\t"
    "v_mfma_f32_16x16x32_bf16 v[28:31], a[44:47], v[168:171], v[28:31]\n\t"
    "ds_read_b128 a[4:7], v0 offset:25600\n\t"
    "v_cvt_pk_bf16_f32 v45, v34, v35\n\t"
    "v_mfma_f32_16x16x32_bf16 v[104:107], a[48:51], v[40:43], v[104:107]\n\t"
    "v_exp_f32 v36, v36\n\t"
    "v_mfma_f32_16x16x32_bf16 v[108:111], a[52:55], v[40:43], v[108:111]\n\t"
    "v_exp_f32 v37, v37\n\t"
    "v_mfma_f32_16x16x32_bf16 v[112:115], a[56:59], v[40:43], v[112:115]\n\t"
    "ds_read_b128 a[8:11], v0 offset:26624\n\t"
    "v_cvt_pk_bf16_f32 v46, v36, v37\n\t"
    "v_mfma_f32_16x16x32_bf16 v[116:119], a[60:63], v[40:43], v[116:119]\n\t"
    "v_exp_f32 v38, v38\n\t"
    "v_mfma_f32_16x16x32_bf16 v[120:123], a[64:67], v[40:43], v[120:123]\n\t"
    "v_exp_f32 v39, v39\n\t"
    "v_mfma_f32_16x16x32_bf16 v[124:127], a[68:71], v[40:43], v[124:127]\n\t"
    "buffer_load_dwordx4 v4, s[16:19], s20 offen offset:2048 sc1 lds\n\t"
    "v_cvt_pk_bf16_f32 v47, v38, v39\n\t"
    "v_mfma_f32_16x16x32_bf16 v[128:131], a[96:99], v[40:43], v[128:131]\n\t"
    "s_add_u32 s20, s20, 12288\n\t"
    "s_add_u32 s22, s22, 12288\n\t"
    "v_mfma_f32_16x16x32_bf16 v[32:35], a[24:27], v[172:175], v[12:15]\n\t"
    "s_mov_b32 m0, s22\n\t"
    "v_exp_f32 v24, v24\n\t"
    "v_mfma_f32_16x16x32_bf16 v[36:39], a[36:39], v[172:175], v[12:15]\n\t"
    "v_exp_f32 v25, v25\n\t"
    "v_mfma_f32_16x16x32_bf16 v[32:35], a[28:31], v[176:179], v[32:35]\n\t"
    "ds_read_b128 a[12:15], v0 offset:27648\n\t"
    "v_cvt_pk_bf16_f32 v40, v24, v25\n\t"
    "v_mfma_f32_16x16x32_bf16 v[36:39], a[40:43], v[176:179], v[36:39]\n\t"
    "v_exp_f32 v26, v26\n\t"
    "v_mfma_f32_16x16x32_bf16 v[32:35], a[32:35], v[180:183], v[32:35]\n\t"
    "v_exp_f32 v27, v27\n\t"
    "v_mfma_f32_16x16x32_bf16 v[36:39], a[44:47], v[180:183], v[36:39]\n\t"
    "ds_read_b128 a[16:19], v0 offset:28672\n\t"
    "v_cvt_pk_bf16_f32 v41, v26, v27\n\t"
    "v_mfma_f32_16x16x32_bf16 v[132:135], a[48:51], v[44:47], v[132:135]\n\t"
    "v_exp_f32 v28, v28\n\t"
    "v_mfma_f32_16x16x32_bf16 v[136:139], a[52:55], v[44:47], v[136:139]\n\t"
    "v_exp_f32 v29, v29\n\t"
    "v_mfma_f32_16x16x32_bf16 v[140:143], a[56:59], v[44:47], v[140:143]\n\t"
    "ds_read_b128 a[20:23], v0 offset:29696\n\t"
    "v_cvt_pk_bf16_f32 v42, v28, v29\n\t"
    "v_mfma_f32_16x16x32_bf16 v[144:147], a[60:63], v[44:47], v[144:147]\n\t"
    "v_exp_f32 v30, v30\n\t"
    "v_mfma_f32_16x16x32_bf16 v[148:151], a[64:67], v[44:47], v[148:151]\n\t"
    "v_exp_f32 v31, v31\n\t"
    "v_mfma_f32_16x16x32_bf16 v[152:155], a[68:71], v[44:47], v[152:155]\n\t"
    "buffer_load_dwordx4 v4, s[16:19], s20 offen offset:0 sc1 lds\n\t"
    "v_cvt_pk_bf16_f32 v43, v30, v31\n\t"
    "v_mfma_f32_16x16x32_bf16 v[156:159], a[96:99], v[44:47], v[156:159]\n\t"
    "v_mfma_f32_16x16x32_bf16 v[24:27], a[24:27], v[184:187], v[16:19]\n\t"
    "v_exp_f32 v32, v32\n\t"
    "v_mfma_f32_16x16x32_bf16 v[28:31], a[36:39], v[184:187], v[16:19]\n\t"
    "v_exp_f32 v33, v33\n\t"
    "v_mfma_f32_16x16x32_bf16 v[24:27], a[28:31], v[188:191], v[24:27]\n\t"
    "ds_read2st64_b64 a[48:51], v1 offset0:60 offset1:61\n\t"
    "v_cvt_pk_bf16_f32 v44, v32, v33\n\t"
    "v_mfma_f32_16x16x32_bf16 v[28:31], a[40:43], v[188:191], v[28:31]\n\t"
    "v_exp_f32 v34, v34\n\t"
    "v_mfma_f32_16x16x32_bf16 v[24:27], a[32:35], v[192:195], v[24:27]\n\t"
    "v_exp_f32 v35, v35\n\t"
    "v_mfma_f32_16x16x32_bf16 v[28:31], a[44:47], v[192:195], v[28:31]\n\t"
    "s_waitcnt lgkmcnt(7)\n\t"
    "ds_read2st64_b64 a[52:55], v1 offset0:62 offset1:63\n\t"
    "v_cvt_pk_bf16_f32 v45, v34, v35\n\t"
    "v_mfma_f32_16x16x32_bf16 v[48:51], a[72:75], v[40:43], v[48:51]\n\t"
    "v_exp_f32 v36, v36\n\t"
    "v_mfma_f32_16x16x32_bf16 v[52:55], a[76:79], v[40:43], v[52:55]\n\t"
    "v_exp_f32 v37, v37\n\t"
    "v_mfma_f32_16x16x32_bf16 v[56:59], a[80:83], v[40:43], v[56:59]\n\t"
    "ds_read2st64_b64 a[56:59], v1 offset0:64 offset1:65\n\t"
    "v_cvt_pk_bf16_f32 v46, v36, v37\n\t"
    "v_mfma_f32_16x16x32_bf16 v[60:63], a[84:87], v[40:43], v[60:63]\n\t"
    "v_exp_f32 v38, v38\n\t"
    "v_mfma_f32_16x16x32_bf16 v[64:67], a[88:91], v[40:43], v[64:67]\n\t"
    "v_exp_f32 v39, v39\n\t"
    "v_mfma_f32_16x16x32_bf16 v[68:71], a[92:95], v[40:43], v[68:71]\n\t"
    "ds_read2st64_b64 a[60:63], v1 offset0:66 offset1:67\n\t"
    "v_cvt_pk_bf16_f32 v47, v38, v39\n\t"
    "v_mfma_f32_16x16x32_bf16 v[72:75], a[96:99], v[40:43], v[72:75]\n\t"
    "ds_read2st64_b64 a[64:67], v1 offset0:68 offset1:69\n\t"
    "buffer_load_dwordx4 v4, s[16:19], s20 offen offset:1024 sc1 lds\n\t"
    "v_mfma_f32_16x16x32_bf16 v[32:35], a[24:27], v[196:199], v[20:23]\n\t"
    "v_exp_f32 v24, v24\n\t"
    "v_mfma_f32_16x16x32_bf16 v[36:39], a[36:39], v[196:199], v[20:23]\n\t"
    "v_exp_f32 v25, v25\n\t"
    "v_mfma_f32_16x16x32_bf16 v[32:35], a[28:31], v[200:203], v[32:35]\n\t"
    "ds_read2st64_b64 a[68:71], v1 offset0:70 offset1:71\n\t"
    "v_cvt_pk_bf16_f32 v40, v24, v25\n\t"
    "v_mfma_f32_16x16x32_bf16 v[36:39], a[40:43], v[200:203], v[36:39]\n\t"
    "v_exp_f32 v26, v26\n\t"
    "v_mfma_f32_16x16x32_bf16 v[32:35], a[32:35], v[204:207], v[32:35]\n\t"
    "v_exp_f32 v27, v27\n\t"
    "v_mfma_f32_16x16x32_bf16 v[36:39], a[44:47], v[204:207], v[36:39]\n\t"
    "buffer_load_dwordx4 v4, s[16:19], s20 offen offset:2048 sc1 lds\n\t"
    "v_cvt_pk_bf16_f32 v41, v26, v27\n\t"
    "v_mfma_f32_16x16x32_bf16 v[76:79], a[72:75], v[44:47], v[76:79]\n\t"
    "s_add_u32 s20, s20, 12288\n\t"
    "v_exp_f32 v28, v28\n\t"
    "v_mfma_f32_16x16x32_bf16 v[80:83], a[76:79], v[44:47], v[80:83]\n\t"
    "s_add_u32 s22, s22, 12288\n\t"
    "v_exp_f32 v29, v29\n\t"
    "v_mfma_f32_16x16x32_bf16 v[84:87], a[80:83], v[44:47], v[84:87]\n\t"
    "v_cvt_pk_bf16_f32 v42, v28, v29\n\t"
    "v_mfma_f32_16x16x32_bf16 v[88:91], a[84:87], v[44:47], v[88:91]\n\t"
    "v_exp_f32 v30, v30\n\t"
    "v_mfma_f32_16x16x32_bf16 v[92:95], a[88:91], v[44:47], v[92:95]\n\t"
    "v_exp_f32 v31, v31\n\t"
    "v_mfma_f32_16x16x32_bf16 v[96:99], a[92:95], v[44:47], v[96:99]\n\t"
    "v_cvt_pk_bf16_f32 v43, v30, v31\n\t"
    "v_mfma_f32_16x16x32_bf16 v[100:103], a[96:99], v[44:47], v[100:103]\n\t"
    "s_waitcnt lgkmcnt(6)\n\t"
    "v_mfma_f32_16x16x32_bf16 v[24:27], a[0:3], v[160:163], v[8:11]\n\t"
    "v_exp_f32 v32, v32\n\t"
    "v_mfma_f32_16x16x32_bf16 v[28:31], a[12:15], v[160:163], v[8:11]\n\t"
    "v_exp_f32 v33, v33\n\t"
    "v_mfma_f32_16x16x32_bf16 v[24:27], a[4:7], v[164:167], v[24:27]\n\t"
    "ds_read_b128 a[24:27], v0 offset:36864\n\t"
    "v_cvt_pk_bf16_f32 v44, v32, v33\n\t"
    "v_mfma_f32_16x16x32_bf16 v[28:31], a[16:19], v[164:167], v[28:31]\n\t"
    "v_exp_f32 v34, v34\n\t"
    "v_mfma_f32_16x16x32_bf16 v[24:27], a[8:11], v[168:171], v[24:27]\n\t"
    "v_exp_f32 v35, v35\n\t"
    "v_mfma_f32_16x16x32_bf16 v[28:31], a[20:23], v[168:171], v[28:31]\n\t"
    "ds_read_b128 a[28:31], v0 offset:37888\n\t"
    "v_cvt_pk_bf16_f32 v45, v34, v35\n\t"
    "v_mfma_f32_16x16x32_bf16 v[104:107], a[72:75], v[40:43], v[104:107]\n\t"
    "v_exp_f32 v36, v36\n\t"
    "v_mfma_f32_16x16x32_bf16 v[108:111], a[76:79], v[40:43], v[108:111]\n\t"
    "v_exp_f32 v37, v37\n\t"
    "v_mfma_f32_16x16x32_bf16 v[112:115], a[80:83], v[40:43], v[112:115]\n\t"
    "ds_read_b128 a[32:35], v0 offset:38912\n\t"
    "v_cvt_pk_bf16_f32 v46, v36, v37\n\t"
    "v_mfma_f32_16x16x32_bf16 v[116:119], a[84:87], v[40:43], v[116:119]\n\t"
    "v_exp_f32 v38, v38\n\t"
    "v_mfma_f32_16x16x32_bf16 v[120:123], a[88:91], v[40:43], v[120:123]\n\t"
    "v_exp_f32 v39, v39\n\t"
    "v_mfma_f32_16x16x32_bf16 v[124:127], a[92:95], v[40:43], v[124:127]\n\t"
    "v_cvt_pk_bf16_f32 v47, v38, v39\n\t"
    "v_mfma_f32_16x16x32_bf16 v[128:131], a[96:99], v[40:43], v[128:131]\n\t"
    "v_mfma_f32_16x16x32_bf16 v[32:35], a[0:3], v[172:175], v[12:15]\n\t"
    "v_exp_f32 v24, v24\n\t"
    "v_mfma_f32_16x16x32_bf16 v[36:39], a[12:15], v[172:175], v[12:15]\n\t"
    "v_exp_f32 v25, v25\n\t"
    "v_mfma_f32_16x16x32_bf16 v[32:35], a[4:7], v[176:179], v[32:35]\n\t"
    "ds_read_b128 a[36:39], v0 offset:39936\n\t"
    "v_cvt_pk_bf16_f32 v40, v24, v25\n\t"
    "v_mfma_f32_16x16x32_bf16 v[36:39], a[16:19], v[176:179], v[36:39]\n\t"
    "v_exp_f32 v26, v26\n\t"
    "v_mfma_f32_16x16x32_bf16 v[32:35], a[8:11], v[180:183], v[32:35]\n\t"
    "v_exp_f32 v27, v27\n\t"
    "v_mfma_f32_16x16x32_bf16 v[36:39], a[20:23], v[180:183], v[36:39]\n\t"
    "ds_read_b128 a[40:43], v0 offset:40960\n\t"
    "v_cvt_pk_bf16_f32 v41, v26, v27\n\t"
    "v_mfma_f32_16x16x32_bf16 v[132:135], a[72:75], v[44:47], v[132:135]\n\t"
    "v_exp_f32 v28, v28\n\t"
    "v_mfma_f32_16x16x32_bf16 v[136:139], a[76:79], v[44:47], v[136:139]\n\t"
    "v_exp_f32 v29, v29\n\t"
    "v_mfma_f32_16x16x32_bf16 v[140:143], a[80:83], v[44:47], v[140:143]\n\t"
    "ds_read_b128 a[44:47], v0 offset:41984\n\t"
    "v_cvt_pk_bf16_f32 v42, v28, v29\n\t"
    "v_mfma_f32_16x16x32_bf16 v[144:147], a[84:87], v[44:47], v[144:147]\n\t"
    "v_exp_f32 v30, v30\n\t"
    "v_mfma_f32_16x16x32_bf16 v[148:151], a[88:91], v[44:47], v[148:151]\n\t"
    "v_exp_f32 v31, v31\n\t"
    "v_mfma_f32_16x16x32_bf16 v[152:155], a[92:95], v[44:47], v[152:155]\n\t"
    "v_cvt_pk_bf16_f32 v43, v30, v31\n\t"
    "v_mfma_f32_16x16x32_bf16 v[156:159], a[96:99], v[44:47], v[156:159]\n\t"
    "v_mfma_f32_16x16x32_bf16 v[24:27], a[0:3], v[184:187], v[16:19]\n\t"
    "v_exp_f32 v32, v32\n\t"
    "v_mfma_f32_16x16x32_bf16 v[28:31], a[12:15], v[184:187], v[16:19]\n\t"
    "v_exp_f32 v33, v33\n\t"
    "v_mfma_f32_16x16x32_bf16 v[24:27], a[4:7], v[188:191], v[24:27]\n\t"
    "ds_read2st64_b64 a[72:75], v1 offset0:84 offset1:85\n\t"
    "v_cvt_pk_bf16_f32 v44, v32, v33\n\t"
    "v_mfma_f32_16x16x32_bf16 v[28:31], a[16:19], v[188:191], v[28:31]\n\t"
    "v_exp_f32 v34, v34\n\t"
    "v_mfma_f32_16x16x32_bf16 v[24:27], a[8:11], v[192:195], v[24:27]\n\t"
    "v_exp_f32 v35, v35\n\t"
    "v_mfma_f32_16x16x32_bf16 v[28:31], a[20:23], v[192:195], v[28:31]\n\t"
    "s_waitcnt lgkmcnt(7)\n\t"
    "ds_read2st64_b64 a[76:79], v1 offset0:86 offset1:87\n\t"
    "v_cvt_pk_bf16_f32 v45, v34, v35\n\t"
    "v_mfma_f32_16x16x32_bf16 v[48:51], a[48:51], v[40:43], v[48:51]\n\t"
    "v_exp_f32 v36, v36\n\t"
    "v_mfma_f32_16x16x32_bf16 v[52:55], a[52:55], v[40:43], v[52:55]\n\t"
    "v_exp_f32 v37, v37\n\t"
    "v_mfma_f32_16x16x32_bf16 v[56:59], a[56:59], v[40:43], v[56:59]\n\t"
    "ds_read2st64_b64 a[80:83], v1 offset0:88 offset1:89\n\t"
    "v_cvt_pk_bf16_f32 v46, v36, v37\n\t"
    "v_mfma_f32_16x16x32_bf16 v[60:63], a[60:63], v[40:43], v[60:63]\n\t"
    "v_exp_f32 v38, v38\n\t"
    "v_mfma_f32_16x16x32_bf16 v[64:67], a[64:67], v[40:43], v[64:67]\n\t"
    "v_exp_f32 v39, v39\n\t"
    "v_mfma_f32_16x16x32_bf16 v[68:71], a[68:71], v[40:43], v[68:71]\n\t"
    "ds_read2st64_b64 a[84:87], v1 offset0:90 offset1:91\n\t"
    "v_cvt_pk_bf16_f32 v47, v38, v39\n\t"
    "v_mfma_f32_16x16x32_bf16 v[72:75], a[96:99], v[40:43], v[72:75]\n\t"
    "ds_read2st64_b64 a[88:91], v1 offset0:92 offset1:93\n\t"
    "v_mfma_f32_16x16x32_bf16 v[32:35], a[0:3], v[196:199], v[20:23]\n\t"
    "s_xor_b32 s23, s23, 49152\n\t"
    "v_exp_f32 v24, v24\n\t"
    "v_mfma_f32_16x16x32_bf16 v[36:39], a[12:15], v[196:199], v[20:23]\n\t"
    "s_add_u32 s22, s23, s25\n\t"
    "v_exp_f32 v25, v25\n\t"
    "v_mfma_f32_16x16x32_bf16 v[32:35], a[4:7], v[200:203], v[32:35]\n\t"
    "s_sub_u32 s24, s24, 1\n\t"
    "ds_read2st64_b64 a[92:95], v1 offset0:94 offset1:95\n\t"
    "v_cvt_pk_bf16_f32 v40, v24, v25\n\t"
    "v_mfma_f32_16x16x32_bf16 v[36:39], a[16:19], v[200:203], v[36:39]\n\t"
    "v_exp_f32 v26, v26\n\t"
    "v_mfma_f32_16x16x32_bf16 v[32:35], a[8:11], v[204:207], v[32:35]\n\t"
    "v_exp_f32 v27, v27\n\t"
    "v_mfma_f32_16x16x32_bf16 v[36:39], a[20:23], v[204:207], v[36:39]\n\t"
    "v_cvt_pk_bf16_f32 v41, v26, v27\n\t"
    "v_xor_b32 v1, 0xc000, v1\n\t"
    "v_mfma_f32_16x16x32_bf16 v[76:79], a[48:51], v[44:47], v[76:79]\n\t"
    "v_exp_f32 v28, v28\n\t"
    "v_mfma_f32_16x16x32_bf16 v[80:83], a[52:55], v[44:47], v[80:83]\n\t"
    "v_exp_f32 v29, v29\n\t"
    "v_mfma_f32_16x16x32_bf16 v[84:87], a[56:59], v[44:47], v[84:87]\n\t"
    "v_cvt_pk_bf16_f32 v42, v28, v29\n\t"
    "v_xor_b32 v0, 0xc000, v0\n\t"
    "v_mfma_f32_16x16x32_bf16 v[88:91], a[60:63], v[44:47], v[88:91]\n\t"
    "v_exp_f32 v30, v30\n\t"
    "v_mfma_f32_16x16x32_bf16 v[92:95], a[64:67], v[44:47], v[92:95]\n\t"
    "v_exp_f32 v31, v31\n\t"
    "v_mfma_f32_16x16x32_bf16 v[96:99], a[68:71], v[44:47], v[96:99]\n\t"
    "v_cvt_pk_bf16_f32 v43, v30, v31\n\t"
    "v_mfma_f32_16x16x32_bf16 v[100:103], a[96:99], v[44:47], v[100:103]\n\t"
    "s_cbranch_scc0 1b\n\t"
    "s_waitcnt vmcnt(0) lgkmcnt(0)\n\t"
    "s_barrier\n\t"
    "s_waitcnt lgkmcnt(6)\n\t"
    "v_mfma_f32_16x16x32_bf16 v[24:27], a[24:27], v[160:163], v[8:11]\n\t"
    "v_exp_f32 v32, v32\n\t"
    "v_mfma_f32_16x16x32_bf16 v[28:31], a[36:39], v[160:163], v[8:11]\n\t"
    "v_exp_f32 v33, v33\n\t"
    "v_mfma_f32_16x16x32_bf16 v[24:27], a[28:31], v[164:167], v[24:27]\n\t"
    "ds_read_b128 a[0:3], v0 offset:0\n\t"
    "v_cvt_pk_bf16_f32 v44, v32, v33\n\t"
    "v_mfma_f32_16x16x32_bf16 v[28:31], a[40:43], v[164:167], v[28:31]\n\t"
    "v_exp_f32 v34, v34\n\t"
    "v_mfma_f32_16x16x32_bf16 v[24:27], a[32:35], v[168:171], v[24:27]\n\t"
    "v_exp_f32 v35, v35\n\t"
    "v_mfma_f32_16x16x32_bf16 v[28:31], a[44:47], v[168:171], v[28:31]\n\t"
    "ds_read_b128 a[4:7], v0 offset:1024\n\t"
    "v_cvt_pk_bf16_f32 v45, v34, v35\n\t"
    "v_mfma_f32_16x16x32_bf16 v[104:107], a[48:51], v[40:43], v[104:107]\n\t"
    "v_exp_f32 v36, v36\n\t"
    "v_mfma_f32_16x16x32_bf16 v[108:111], a[52:55], v[40:43], v[108:111]\n\t"
    "v_exp_f32 v37, v37\n\t"
    "v_mfma_f32_16x16x32_bf16 v[112:115], a[56:59], v[40:43], v[112:115]\n\t"
    "ds_read_b128 a[8:11], v0 offset:2048\n\t"
    "v_cvt_pk_bf16_f32 v46, v36, v37\n\t"
    "v_mfma_f32_16x16x32_bf16 v[116:119], a[60:63], v[40:43], v[116:119]\n\t"
    "v_exp_f32 v38, v38\n\t"
    "v_mfma_f32_16x16x32_bf16 v[120:123], a[64:67], v[40:43], v[120:123]\n\t"
    "v_exp_f32 v39, v39\n\t"
    "v_mfma_f32_16x16x32_bf16 v[124:127], a[68:71], v[40:43], v[124:127]\n\t"
    "ds_read_b128 a[100:103], v0 offset:12288\n\t"
    "v_cvt_pk_bf16_f32 v47, v38, v39\n\t"
    "v_mfma_f32_16x16x32_bf16 v[128:131], a[96:99], v[40:43], v[128:131]\n\t"
    "ds_read_b128 a[104:107], v0 offset:13312\n\t"
    "ds_read_b128 a[108:111], v0 offset:14336\n\t"
    "v_mfma_f32_16x16x32_bf16 v[32:35], a[24:27], v[172:175], v[12:15]\n\t"
    "v_exp_f32 v24, v24\n\t"
    "v_mfma_f32_16x16x32_bf16 v[36:39], a[36:39], v[172:175], v[12:15]\n\t"
    "v_exp_f32 v25, v25\n\t"
    "v_mfma_f32_16x16x32_bf16 v[32:35], a[28:31], v[176:179], v[32:35]\n\t"
    "ds_read_b128 a[112:115], v0 offset:15360\n\t"
    "v_cvt_pk_bf16_f32 v40, v24, v25\n\t"
    "v_mfma_f32_16x16x32_bf16 v[36:39], a[40:43], v[176:179], v[36:39]\n\t"
    "v_exp_f32 v26, v26\n\t"
    "v_mfma_f32_16x16x32_bf16 v[32:35], a[32:35], v[180:183], v[32:35]\n\t"
    "v_exp_f32 v27, v27\n\t"
    "v_mfma_f32_16x16x32_bf16 v[36:39], a[44:47], v[180:183], v[36:39]\n\t"
    "ds_read_b128 a[116:119], v0 offset:16384\n\t"
    "v_cvt_pk_bf16_f32 v41, v26, v27\n\t"
    "v_mfma_f32_16x16x32_bf16 v[132:135], a[48:51], v[44:47], v[132:135]\n\t"
    "v_exp_f32 v28, v28\n\t"
    "v_mfma_f32_16x16x32_bf16 v[136:139], a[52:55], v[44:47], v[136:139]\n\t"
    "v_exp_f32 v29, v29\n\t"
    "v_mfma_f32_16x16x32_bf16 v[140:143], a[56:59], v[44:47], v[140:143]\n\t"
    "ds_read_b128 a[120:123], v0 offset:17408\n\t"
    "v_cvt_pk_bf16_f32 v42, v28, v29\n\t"
    "v_mfma_f32_16x16x32_bf16 v[144:147], a[60:63], v[44:47], v[144:147]\n\t"
    "v_exp_f32 v30, v30\n\t"
    "v_mfma_f32_16x16x32_bf16 v[148:151], a[64:67], v[44:47], v[148:151]\n\t"
    "v_exp_f32 v31, v31\n\t"
    "v_mfma_f32_16x16x32_bf16 v[152:155], a[68:71], v[44:47], v[152:155]\n\t"
    "ds_read_b128 a[124:127], v0 offset:18432\n\t"
    "v_cvt_pk_bf16_f32 v43, v30, v31\n\t"
    "v_mfma_f32_16x16x32_bf16 v[156:159], a[96:99], v[44:47], v[156:159]\n\t"
    "ds_read_b128 a[128:131], v0 offset:19456\n\t"
    "ds_read_b128 a[132:135], v0 offset:20480\n\t"
    "v_mfma_f32_16x16x32_bf16 v[24:27], a[24:27], v[184:187], v[16:19]\n\t"
    "v_exp_f32 v32, v32\n\t"
    "v_mfma_f32_16x16x32_bf16 v[28:31], a[36:39], v[184:187], v[16:19]\n\t"
    "v_exp_f32 v33, v33\n\t"
    "v_mfma_f32_16x16x32_bf16 v[24:27], a[28:31], v[188:191], v[24:27]\n\t"
    "ds_read2st64_b64 a[48:51], v1 offset0:12 offset1:13\n\t"
    "v_cvt_pk_bf16_f32 v44, v32, v33\n\t"
    "v_mfma_f32_16x16x32_bf16 v[28:31], a[40:43], v[188:191], v[28:31]\n\t"
    "v_exp_f32 v34, v34\n\t"
    "v_mfma_f32_16x16x32_bf16 v[24:27], a[32:35], v[192:195], v[24:27]\n\t"
    "v_exp_f32 v35, v35\n\t"
    "v_mfma_f32_16x16x32_bf16 v[28:31], a[44:47], v[192:195], v[28:31]\n\t"
    "s_waitcnt lgkmcnt(13)\n\t"
    "ds_read2st64_b64 a[52:55], v1 offset0:14 offset1:15\n\t"
    "v_cvt_pk_bf16_f32 v45, v34, v35\n\t"
    "v_mfma_f32_16x16x32_bf16 v[48:51], a[72:75], v[40:43], v[48:51]\n\t"
    "v_exp_f32 v36, v36\n\t"
    "v_mfma_f32_16x16x32_bf16 v[52:55], a[76:79], v[40:43], v[52:55]\n\t"
    "v_exp_f32 v37, v37\n\t"
    "v_mfma_f32_16x16x32_bf16 v[56:59], a[80:83], v[40:43], v[56:59]\n\t"
    "ds_read2st64_b64 a[56:59], v1 offset0:16 offset1:17\n\t"
    "v_cvt_pk_bf16_f32 v46, v36, v37\n\t"
    "v_mfma_f32_16x16x32_bf16 v[60:63], a[84:87], v[40:43], v[60:63]\n\t"
    "v_exp_f32 v38, v38\n\t"
    "v_mfma_f32_16x16x32_bf16 v[64:67], a[88:91], v[40:43], v[64:67]\n\t"
    "v_exp_f32 v39, v39\n\t"
    "v_mfma_f32_16x16x32_bf16 v[68:71], a[92:95], v[40:43], v[68:71]\n\t"
    "ds_read2st64_b64 a[60:63], v1 offset0:18 offset1:19\n\t"
    "v_cvt_pk_bf16_f32 v47, v38, v39\n\t"
    "v_mfma_f32_16x16x32_bf16 v[72:75], a[96:99], v[40:43], v[72:75]\n\t"
    "ds_read2st64_b64 a[64:67], v1 offset0:20 offset1:21\n\t"
    "v_mfma_f32_16x16x32_bf16 v[32:35], a[24:27], v[196:199], v[20:23]\n\t"
    "v_exp_f32 v24, v24\n\t"
    "v_mfma_f32_16x16x32_bf16 v[36:39], a[36:39], v[196:199], v[20:23]\n\t"
    "v_exp_f32 v25, v25\n\t"
    "v_mfma_f32_16x16x32_bf16 v[32:35], a[28:31], v[200:203], v[32:35]\n\t"
    "ds_read2st64_b64 a[68:71], v1 offset0:22 offset1:23\n\t"
    "v_cvt_pk_bf16_f32 v40, v24, v25\n\t"
    "v_mfma_f32_16x16x32_bf16 v[36:39], a[40:43], v[200:203], v[36:39]\n\t"
    "v_exp_f32 v26, v26\n\t"
    "v_mfma_f32_16x16x32_bf16 v[32:35], a[32:35], v[204:207], v[32:35]\n\t"
    "v_exp_f32 v27, v27\n\t"
    "v_mfma_f32_16x16x32_bf16 v[36:39], a[44:47], v[204:207], v[36:39]\n\t"
    "ds_read_b128 a[136:139], v0 offset:21504\n\t"
    "v_cvt_pk_bf16_f32 v41, v26, v27\n\t"
    "v_mfma_f32_16x16x32_bf16 v[76:79], a[72:75], v[44:47], v[76:79]\n\t"
    "v_exp_f32 v28, v28\n\t"
    "v_mfma_f32_16x16x32_bf16 v[80:83], a[76:79], v[44:47], v[80:83]\n\t"
    "v_exp_f32 v29, v29\n\t"
    "v_mfma_f32_16x16x32_bf16 v[84:87], a[80:83], v[44:47], v[84:87]\n\t"
    "ds_read_b128 a[140:143], v0 offset:22528\n\t"
    "v_cvt_pk_bf16_f32 v42, v28, v29\n\t"
    "v_mfma_f32_16x16x32_bf16 v[88:91], a[84:87], v[44:47], v[88:91]\n\t"
    "v_exp_f32 v30, v30\n\t"
    "v_mfma_f32_16x16x32_bf16 v[92:95], a[88:91], v[44:47], v[92:95]\n\t"
    "v_exp_f32 v31, v31\n\t"
    "v_mfma_f32_16x16x32_bf16 v[96:99], a[92:95], v[44:47], v[96:99]\n\t"
    "ds_read_b128 a[144:147], v0 offset:23552\n\t"
    "v_cvt_pk_bf16_f32 v43, v30, v31\n\t"
    "v_mfma_f32_16x16x32_bf16 v[100:103], a[96:99], v[44:47], v[100:103]\n\t"
    "ds_read_b128 a[148:151], v0 offset:24576\n\t"
    "ds_read_b128 a[152:155], v0 offset:25600\n\t"
    "s_waitcnt lgkmcnt(15)\n\t"
    "v_mfma_f32_16x16x32_bf16 v[24:27], a[0:3], v[160:163], v[8:11]\n\t"
    "v_exp_f32 v32, v32\n\t"
    "v_mfma_f32_16x16x32_bf16 v[24:27], a[4:7], v[164:167], v[24:27]\n\t"
    "v_exp_f32 v33, v33\n\t"
    "v_mfma_f32_16x16x32_bf16 v[24:27], a[8:11], v[168:171], v[24:27]\n\t"
    "v_cvt_pk_bf16_f32 v44, v32, v33\n\t"
    "v_exp_f32 v34, v34\n\t"
    "v_mfma_f32_16x16x32_bf16 v[104:107], a[72:75], v[40:43], v[104:107]\n\t"
    "v_exp_f32 v35, v35\n\t"
    "v_mfma_f32_16x16x32_bf16 v[108:111], a[76:79], v[40:43], v[108:111]\n\t"
    "ds_read_b128 a[156:159], v0 offset:26624\n\t"
    "v_cvt_pk_bf16_f32 v45, v34, v35\n\t"
    "v_mfma_f32_16x16x32_bf16 v[112:115], a[80:83], v[40:43], v[112:115]\n\t"
    "v_exp_f32 v36, v36\n\t"
    "v_mfma_f32_16x16x32_bf16 v[116:119], a[84:87], v[40:43], v[116:119]\n\t"
    "v_exp_f32 v37, v37\n\t"
    "v_mfma_f32_16x16x32_bf16 v[120:123], a[88:91], v[40:43], v[120:123]\n\t"
    "v_cvt_pk_bf16_f32 v46, v36, v37\n\t"
    "v_exp_f32 v38, v38\n\t"
    "v_mfma_f32_16x16x32_bf16 v[124:127], a[92:95], v[40:43], v[124:127]\n\t"
    "v_exp_f32 v39, v39\n\t"
    "v_mfma_f32_16x16x32_bf16 v[128:131], a[96:99], v[40:43], v[128:131]\n\t"
    "ds_read_b128 a[160:163], v0 offset:27648\n\t"
    "ds_read_b128 a[164:167], v0 offset:28672\n\t"
    "ds_read_b128 a[168:171], v0 offset:29696\n\t"
    "v_cvt_pk_bf16_f32 v47, v38, v39\n\t"
    "v_mfma_f32_16x16x32_bf16 v[32:35], a[0:3], v[172:175], v[12:15]\n\t"
    "v_exp_f32 v24, v24\n\t"
    "v_mfma_f32_16x16x32_bf16 v[32:35], a[4:7], v[176:179], v[32:35]\n\t"
    "v_mfma_f32_16x16x32_bf16 v[32:35], a[8:11], v[180:183], v[32:35]\n\t"
    "v_exp_f32 v25, v25\n\t"
    "v_mfma_f32_16x16x32_bf16 v[132:135], a[72:75], v[44:47], v[132:135]\n\t"
    "v_cvt_pk_bf16_f32 v40, v24, v25\n\t"
    "v_mfma_f32_16x16x32_bf16 v[136:139], a[76:79], v[44:47], v[136:139]\n\t"
    "v_mfma_f32_16x16x32_bf16 v[140:143], a[80:83], v[44:47], v[140:143]\n\t"
    "v_exp_f32 v26, v26\n\t"
    "v_mfma_f32_16x16x32_bf16 v[144:147], a[84:87], v[44:47], v[144:147]\n\t"
    "v_mfma_f32_16x16x32_bf16 v[148:151], a[88:91], v[44:47], v[148:151]\n\t"
    "v_exp_f32 v27, v27\n\t"
    "v_mfma_f32_16x16x32_bf16 v[152:155], a[92:95], v[44:47], v[152:155]\n\t"
    "v_cvt_pk_bf16_f32 v41, v26, v27\n\t"
    "v_mov_b32 v42, 0\n\t"
    "v_mov_b32 v43, 0\n\t"
    "v_mfma_f32_16x16x32_bf16 v[156:159], a[96:99], v[44:47], v[156:159]\n\t"
    "v_mfma_f32_16x16x32_bf16 v[24:27], a[0:3], v[184:187], v[16:19]\n\t"
    "v_exp_f32 v32, v32\n\t"
    "v_mfma_f32_16x16x32_bf16 v[24:27], a[4:7], v[188:191], v[24:27]\n\t"
    "v_mfma_f32_16x16x32_bf16 v[24:27], a[8:11], v[192:195], v[24:27]\n\t"
    "s_waitcnt lgkmcnt(9)\n\t"
    "v_exp_f32 v33, v33\n\t"
    "v_mfma_f32_16x16x32_bf16 v[48:51], a[48:51], v[40:43], v[48:51]\n\t"
    "v_cvt_pk_bf16_f32 v44, v32, v33\n\t"
    "v_mfma_f32_16x16x32_bf16 v[52:55], a[52:55], v[40:43], v[52:55]\n\t"
    "v_mfma_f32_16x16x32_bf16 v[56:59], a[56:59], v[40:43], v[56:59]\n\t"
    "v_exp_f32 v34, v34\n\t"
    "v_mfma_f32_16x16x32_bf16 v[60:63], a[60:63], v[40:43], v[60:63]\n\t"
    "v_mfma_f32_16x16x32_bf16 v[64:67], a[64:67], v[40:43], v[64:67]\n\t"
    "v_exp_f32 v35, v35\n\t"
    "v_mfma_f32_16x16x32_bf16 v[68:71], a[68:71], v[40:43], v[68:71]\n\t"
    "v_cvt_pk_bf16_f32 v45, v34, v35\n\t"
    "v_mov_b32 v46, 0\n\t"
    "v_mov_b32 v47, 0\n\t"
    "v_mfma_f32_16x16x32_bf16 v[72:75], a[96:99], v[40:43], v[72:75]\n\t"
    "v_mfma_f32_16x16x32_bf16 v[32:35], a[0:3], v[196:199], v[20:23]\n\t"
    "v_exp_f32 v24, v24\n\t"
    "v_mfma_f32_16x16x32_bf16 v[32:35], a[4:7], v[200:203], v[32:35]\n\t"
    "v_mfma_f32_16x16x32_bf16 v[32:35], a[8:11], v[204:207], v[32:35]\n\t"
    "v_exp_f32 v25, v25\n\t"
    "v_mfma_f32_16x16x32_bf16 v[76:79], a[48:51], v[44:47], v[76:79]\n\t"
    "v_cvt_pk_bf16_f32 v40, v24, v25\n\t"
    "v_mfma_f32_16x16x32_bf16 v[80:83], a[52:55], v[44:47], v[80:83]\n\t"
    "v_mfma_f32_16x16x32_bf16 v[84:87], a[56:59], v[44:47], v[84:87]\n\t"
    "v_exp_f32 v26, v26\n\t"
    "v_mfma_f32_16x16x32_bf16 v[88:91], a[60:63], v[44:47], v[88:91]\n\t"
    "v_mfma_f32_16x16x32_bf16 v[92:95], a[64:67], v[44:47], v[92:95]\n\t"
    "v_exp_f32 v27, v27\n\t"
    "v_mfma_f32_16x16x32_bf16 v[96:99], a[68:71], v[44:47], v[96:99]\n\t"
    "v_cvt_pk_bf16_f32 v41, v26, v27\n\t"
    "v_mov_b32 v42, 0\n\t"
    "v_mov_b32 v43, 0\n\t"
    "v_mfma_f32_16x16x32_bf16 v[100:103], a[96:99], v[44:47], v[100:103]\n\t"
    "s_nop 0\n\t"
    "v_mfma_f32_16x16x32_bf16 v[104:107], a[48:51], v[40:43], v[104:107]\n\t"
    "v_exp_f32 v32, v32\n\t"
    "v_mfma_f32_16x16x32_bf16 v[108:111], a[52:55], v[40:43], v[108:111]\n\t"
    "v_exp_f32 v33, v33\n\t"
    "v_mfma_f32_16x16x32_bf16 v[112:115], a[56:59], v[40:43], v[112:115]\n\t"
    "v_cvt_pk_bf16_f32 v44, v32, v33\n\t"
    "v_cvt_pk_bf16_f32 v48, v48, v49\n\t"
    "v_mfma_f32_16x16x32_bf16 v[116:119], a[60:63], v[40:43], v[116:119]\n\t"
    "v_exp_f32 v34, v34\n\t"
    "v_mfma_f32_16x16x32_bf16 v[120:123], a[64:67], v[40:43], v[120:123]\n\t"
    "v_cvt_pk_bf16_f32 v49, v50, v51\n\t"
    "v_cvt_pk_bf16_f32 v50, v52, v53\n\t"
    "v_cvt_pk_bf16_f32 v51, v54, v55\n\t"
    "v_mfma_f32_16x16x32_bf16 v[124:127], a[68:71], v[40:43], v[124:127]\n\t"
    "v_exp_f32 v35, v35\n\t"
    "v_cvt_pk_bf16_f32 v56, v56, v57\n\t"
    "v_cvt_pk_bf16_f32 v57, v58, v59\n\t"
    "v_mfma_f32_16x16x32_bf16 v[128:131], a[96:99], v[40:43], v[128:131]\n\t"
    "v_cvt_pk_bf16_f32 v45, v34, v35\n\t"
    "v_mov_b32 v46, 0\n\t"
    "v_mov_b32 v47, 0\n\t"
    "s_nop 1\n\t"
    "v_mfma_f32_16x16x32_bf16 v[132:135], a[48:51], v[44:47], v[132:135]\n\t"
    "v_cvt_pk_bf16_f32 v58, v60, v61\n\t"
    "v_cvt_pk_bf16_f32 v59, v62, v63\n\t"
    "v_mfma_f32_16x16x32_bf16 v[136:139], a[52:55], v[44:47], v[136:139]\n\t"
    "v_cvt_pk_bf16_f32 v64, v64, v65\n\t"
    "v_cvt_pk_bf16_f32 v65, v66, v67\n\t"
    "v_mfma_f32_16x16x32_bf16 v[140:143], a[56:59], v[44:47], v[140:143]\n\t"
    "v_cvt_pk_bf16_f32 v66, v68, v69\n\t"
    "v_cvt_pk_bf16_f32 v67, v70, v71\n\t"
    "v_mfma_f32_16x16x32_bf16 v[144:147], a[60:63], v[44:47], v[144:147]\n\t"
    "v_rcp_f32 v73, v72\n\t"
    "v_mfma_f32_16x16x32_bf16 v[148:151], a[64:67], v[44:47], v[148:151]\n\t"
    "v_cvt_pk_bf16_f32 v76, v76, v77\n\t"
    "v_cvt_pk_bf16_f32 v77, v78, v79\n\t"
    "v_mfma_f32_16x16x32_bf16 v[152:155], a[68:71], v[44:47], v[152:155]\n\t"
    "v_cvt_pk_bf16_f32 v78, v80, v81\n\t"
    "v_cvt_pk_bf16_f32 v79, v82, v83\n\t"
    "v_mfma_f32_16x16x32_bf16 v[156:159], a[96:99], v[44:47], v[156:159]\n\t"
    "v_cvt_pk_bf16_f32 v84, v84, v85\n\t"
    "v_cvt_pk_bf16_f32 v85, v86, v87\n\t"
    "s_waitcnt lgkmcnt(0)\n\t"
    "v_mfma_f32_16x16x32_bf16 v[8:11], a[100:103], v[48:51], 0\n\t"
    "v_cvt_pk_bf16_f32 v86, v88, v89\n\t"
    "v_cvt_pk_bf16_f32 v87, v90, v91\n\t"
    "v_cvt_pk_bf16_f32 v92, v92, v93\n\t"
    "v_mfma_f32_16x16x32_bf16 v[12:15], a[112:115], v[48:51], 0\n\t"
    "v_cvt_pk_bf16_f32 v93, v94, v95\n\t"
    "v_cvt_pk_bf16_f32 v94, v96, v97\n\t"
    "v_cvt_pk_bf16_f32 v95, v98, v99\n\t"
    "v_mfma_f32_16x16x32_bf16 v[8:11], a[104:107], v[56:59], v[8:11]\n\t"
    "v_rcp_f32 v101, v100\n\t"
    "v_cvt_pk_bf16_f32 v104, v104, v105\n\t"
    "v_mfma_f32_16x16x32_bf16 v[12:15], a[116:119], v[56:59], v[12:15]\n\t"
    "v_cvt_pk_bf16_f32 v105, v106, v107\n\t"
    "v_cvt_pk_bf16_f32 v106, v108, v109\n\t"
    "v_cvt_pk_bf16_f32 v107, v110, v111\n\t"
    "v_mfma_f32_16x16x32_bf16 v[8:11], a[108:111], v[64:67], v[8:11]\n\t"
    "v_cvt_pk_bf16_f32 v112, v112, v113\n\t"
    "v_cvt_pk_bf16_f32 v113, v114, v115\n\t"
    "v_cvt_pk_bf16_f32 v114, v116, v117\n\t"
    "v_mfma_f32_16x16x32_bf16 v[12:15], a[120:123], v[64:67], v[12:15]\n\t"
    "v_cvt_pk_bf16_f32 v115, v118, v119\n\t"
    "v_cvt_pk_bf16_f32 v120, v120, v121\n\t"
    "v_cvt_pk_bf16_f32 v121, v122, v123\n\t"
    "v_mfma_f32_16x16x32_bf16 v[16:19], a[124:127], v[48:51], 0\n\t"
    "v_mul_f32 v8, v8, v73\n\t"
    "v_mul_f32 v9, v9, v73\n\t"
    "v_mul_f32 v10, v10, v73\n\t"
    "v_mfma_f32_16x16x32_bf16 v[20:23], a[136:139], v[48:51], 0\n\t"
    "v_mul_f32 v11, v11, v73\n\t"
    "v_mul_f32 v12, v12, v73\n\t"
    "v_mul_f32 v13, v13, v73\n\t"
    "v_mfma_f32_16x16x32_bf16 v[16:19], a[128:131], v[56:59], v[16:19]\n\t"
    "v_mul_f32 v14, v14, v73\n\t"
    "v_mul_f32 v15, v15, v73\n\t"
    "v_cvt_pk_bf16_f32 v8, v8, v9\n\t"
    "v_mfma_f32_16x16x32_bf16 v[20:23], a[140:143], v[56:59], v[20:23]\n\t"
    "v_cvt_pk_bf16_f32 v9, v10, v11\n\t"
    "v_cvt_pk_bf16_f32 v10, v12, v13\n\t"
    "v_cvt_pk_bf16_f32 v11, v14, v15\n\t"
    "v_mfma_f32_16x16x32_bf16 v[16:19], a[132:135], v[64:67], v[16:19]\n\t"
    "global_store_dwordx4 v3, v[8:11], s[50:51] offset:0 sc1\n\t"
    "v_cvt_pk_bf16_f32 v122, v124, v125\n\t"
    "v_cvt_pk_bf16_f32 v123, v126, v127\n\t"
    "v_mfma_f32_16x16x32_bf16 v[20:23], a[144:147], v[64:67], v[20:23]\n\t"
    "v_rcp_f32 v129, v128\n\t"
    "v_cvt_pk_bf16_f32 v132, v132, v133\n\t"
    "v_mfma_f32_16x16x32_bf16 v[24:27], a[148:151], v[48:51], 0\n\t"
    "s_nop 0\n\t"
    "v_mul_f32 v16, v16, v73\n\t"
    "v_mul_f32 v17, v17, v73\n\t"
    "v_mul_f32 v18, v18, v73\n\t"
    "v_mfma_f32_16x16x32_bf16 v[28:31], a[160:163], v[48:51], 0\n\t"
    "v_mul_f32 v19, v19, v73\n\t"
    "v_mul_f32 v20, v20, v73\n\t"
    "v_mul_f32 v21, v21, v73\n\t"
    "v_mfma_f32_16x16x32_bf16 v[24:27], a[152:155], v[56:59], v[24:27]\n\t"
    "v_mul_f32 v22, v22, v73\n\t"
    "v_mul_f32 v23, v23, v73\n\t"
    "v_cvt_pk_bf16_f32 v16, v16, v17\n\t"
    "v_mfma_f32_16x16x32_bf16 v[28:31], a[164:167], v[56:59], v[28:31]\n\t"
    "v_cvt_pk_bf16_f32 v17, v18, v19\n\t"
    "v_cvt_pk_bf16_f32 v18, v20, v21\n\t"
    "v_cvt_pk_bf16_f32 v19, v22, v23\n\t"
    "v_mfma_f32_16x16x32_bf16 v[24:27], a[156:159], v[64:67], v[24:27]\n\t"
    "global_store_dwordx4 v3, v[16:19], s[50:51] offset:64 sc1\n\t"
    "v_cvt_pk_bf16_f32 v133, v134, v135\n\t"
    "v_cvt_pk_bf16_f32 v134, v136, v137\n\t"
    "v_mfma_f32_16x16x32_bf16 v[28:31], a[168:171], v[64:67], v[28:31]\n\t"
    "v_cvt_pk_bf16_f32 v135, v138, v139\n\t"
    "v_cvt_pk_bf16_f32 v140, v140, v141\n\t"
    "v_cvt_pk_bf16_f32 v141, v142, v143\n\t"
    "v_mfma_f32_16x16x32_bf16 v[212:215], a[100:103], v[76:79], 0\n\t"
    "v_mul_f32 v24, v24, v73\n\t"
    "v_mul_f32 v25, v25, v73\n\t"
    "v_mul_f32 v26, v26, v73\n\t"
    "v_mfma_f32_16x16x32_bf16 v[216:219], a[112:115], v[76:79], 0\n\t"
    "v_mul_f32 v27, v27, v73\n\t"
    "v_mul_f32 v28, v28, v73\n\t"
    "v_mul_f32 v29, v29, v73\n\t"
    "v_mfma_f32_16x16x32_bf16 v[212:215], a[104:107], v[84:87], v[212:215]\n\t"
    "v_mul_f32 v30, v30, v73\n\t"
    "v_mul_f32 v31, v31, v73\n\t"
    "v_cvt_pk_bf16_f32 v24, v24, v25\n\t"
    "v_mfma_f32_16x16x32_bf16 v[216:219], a[116:119], v[84:87], v[216:219]\n\t"
    "v_cvt_pk_bf16_f32 v25, v26, v27\n\t"
    "v_cvt_pk_bf16_f32 v26, v28, v29\n\t"
    "v_cvt_pk_bf16_f32 v27, v30, v31\n\t"
    "v_mfma_f32_16x16x32_bf16 v[212:215], a[108:111], v[92:95], v[212:215]\n\t"
    "global_store_dwordx4 v3, v[24:27], s[50:51] offset:128 sc1\n\t"
    "v_add_u32 v3, 86016, v3\n\t"
    "v_cvt_pk_bf16_f32 v142, v144, v145\n\t"
    "v_mfma_f32_16x16x32_bf16 v[216:219], a[120:123], v[92:95], v[216:219]\n\t"
    "v_cvt_pk_bf16_f32 v143, v146, v147\n\t"
    "v_cvt_pk_bf16_f32 v148, v148, v149\n\t"
    "v_cvt_pk_bf16_f32 v149, v150, v151\n\t"
    "v_mfma_f32_16x16x32_bf16 v[220:223], a[124:127], v[76:79], 0\n\t"
    "v_mul_f32 v212, v212, v101\n\t"
    "v_mul_f32 v213, v213, v101\n\t"
    "v_mul_f32 v214, v214, v101\n\t"
    "v_mfma_f32_16x16x32_bf16 v[224:227], a[136:139], v[76:79], 0\n\t"
    "v_mul_f32 v215, v215, v101\n\t"
    "v_mul_f32 v216, v216, v101\n\t"
    "v_mul_f32 v217, v217, v101\n\t"
    "v_mfma_f32_16x16x32_bf16 v[220:223], a[128:131], v[84:87], v[220:223]\n\t"
    "v_mul_f32 v218, v218, v101\n\t"
    "v_mul_f32 v219, v219, v101\n\t"
    "v_cvt_pk_bf16_f32 v212, v212, v213\n\t"
    "v_mfma_f32_16x16x32_bf16 v[224:227], a[140:143], v[84:87], v[224:227]\n\t"
    "v_cvt_pk_bf16_f32 v213, v214, v215\n\t"
    "v_cvt_pk_bf16_f32 v214, v216, v217\n\t"
    "v_cvt_pk_bf16_f32 v215, v218, v219\n\t"
    "v_mfma_f32_16x16x32_bf16 v[220:223], a[132:135], v[92:95], v[220:223]\n\t"
    "global_store_dwordx4 v3, v[212:215], s[50:51] offset:0 sc1\n\t"
    "v_cvt_pk_bf16_f32 v150, v152, v153\n\t"
    "v_cvt_pk_bf16_f32 v151, v154, v155\n\t"
    "v_mfma_f32_16x16x32_bf16 v[224:227], a[144:147], v[92:95], v[224:227]\n\t"
    "v_rcp_f32 v157, v156\n\t"
    "v_mfma_f32_16x16x32_bf16 v[228:231], a[148:151], v[76:79], 0\n\t"
    "s_nop 1\n\t"
    "v_mul_f32 v220, v220, v101\n\t"
    "v_mul_f32 v221, v221, v101\n\t"
    "v_mul_f32 v222, v222, v101\n\t"
    "v_mfma_f32_16x16x32_bf16 v[232:235], a[160:163], v[76:79], 0\n\t"
    "v_mul_f32 v223, v223, v101\n\t"
    "v_mul_f32 v224, v224, v101\n\t"
    "v_mul_f32 v225, v225, v101\n\t"
    "v_mfma_f32_16x16x32_bf16 v[228:231], a[152:155], v[84:87], v[228:231]\n\t"
    "v_mul_f32 v226, v226, v101\n\t"
    "v_mul_f32 v227, v227, v101\n\t"
    "v_cvt_pk_bf16_f32 v220, v220, v221\n\t"
    "v_mfma_f32_16x16x32_bf16 v[232:235], a[164:167], v[84:87], v[232:235]\n\t"
    "v_cvt_pk_bf16_f32 v221, v222, v223\n\t"
    "v_cvt_pk_bf16_f32 v222, v224, v225\n\t"
    "v_cvt_pk_bf16_f32 v223, v226, v227\n\t"
    "v_mfma_f32_16x16x32_bf16 v[228:231], a[156:159], v[92:95], v[228:231]\n\t"
    "global_store_dwordx4 v3, v[220:223], s[50:51] offset:64 sc1\n\t"
    "v_mfma_f32_16x16x32_bf16 v[232:235], a[168:171], v[92:95], v[232:235]\n\t"
    "v_mfma_f32_16x16x32_bf16 v[8:11], a[100:103], v[104:107], 0\n\t"
    "s_nop 4\n\t"
    "v_mul_f32 v228, v228, v101\n\t"
    "v_mul_f32 v229, v229, v101\n\t"
    "v_mul_f32 v230, v230, v101\n\t"
    "v_mfma_f32_16x16x32_bf16 v[12:15], a[112:115], v[104:107], 0\n\t"
    "v_mul_f32 v231, v231, v101\n\t"
    "v_mul_f32 v232, v232, v101\n\t"
    "v_mul_f32 v233, v233, v101\n\t"
    "v_mfma_f32_16x16x32_bf16 v[8:11], a[104:107], v[112:115], v[8:11]\n\t"
    "v_mul_f32 v234, v234, v101\n\t"
    "v_mul_f32 v235, v235, v101\n\t"
    "v_cvt_pk_bf16_f32 v228, v228, v229\n\t"
    "v_mfma_f32_16x16x32_bf16 v[12:15], a[116:119], v[112:115], v[12:15]\n\t"
    "v_cvt_pk_bf16_f32 v229, v230, v231\n\t"
    "v_cvt_pk_bf16_f32 v230, v232, v233\n\t"
    "v_cvt_pk_bf16_f32 v231, v234, v235\n\t"
    "v_mfma_f32_16x16x32_bf16 v[8:11], a[108:111], v[120:123], v[8:11]\n\t"
    "global_store_dwordx4 v3, v[228:231], s[50:51] offset:128 sc1\n\t"
    "v_add_u32 v3, 86016, v3\n\t"
    "v_mfma_f32_16x16x32_bf16 v[12:15], a[120:123], v[120:123], v[12:15]\n\t"
    "v_mfma_f32_16x16x32_bf16 v[16:19], a[124:127], v[104:107], 0\n\t"
    "s_nop 3\n\t"
    "v_mul_f32 v8, v8, v129\n\t"
    "v_mul_f32 v9, v9, v129\n\t"
    "v_mul_f32 v10, v10, v129\n\t"
    "v_mfma_f32_16x16x32_bf16 v[20:23], a[136:139], v[104:107], 0\n\t"
    "v_mul_f32 v11, v11, v129\n\t"
    "v_mul_f32 v12, v12, v129\n\t"
    "v_mul_f32 v13, v13, v129\n\t"
    "v_mfma_f32_16x16x32_bf16 v[16:19], a[128:131], v[112:115], v[16:19]\n\t"
    "v_mul_f32 v14, v14, v129\n\t"
    "v_mul_f32 v15, v15, v129\n\t"
    "v_cvt_pk_bf16_f32 v8, v8, v9\n\t"
    "v_mfma_f32_16x16x32_bf16 v[20:23], a[140:143], v[112:115], v[20:23]\n\t"
    "v_cvt_pk_bf16_f32 v9, v10, v11\n\t"
    "v_cvt_pk_bf16_f32 v10, v12, v13\n\t"
    "v_cvt_pk_bf16_f32 v11, v14, v15\n\t"
    "v_mfma_f32_16x16x32_bf16 v[16:19], a[132:135], v[120:123], v[16:19]\n\t"
    "global_store_dwordx4 v3, v[8:11], s[50:51] offset:0 sc1\n\t"
    "v_mfma_f32_16x16x32_bf16 v[20:23], a[144:147], v[120:123], v[20:23]\n\t"
    "v_mfma_f32_16x16x32_bf16 v[24:27], a[148:151], v[104:107], 0\n\t"
    "s_nop 4\n\t"
    "v_mul_f32 v16, v16, v129\n\t"
    "v_mul_f32 v17, v17, v129\n\t"
    "v_mul_f32 v18, v18, v129\n\t"
    "v_mfma_f32_16x16x32_bf16 v[28:31], a[160:163], v[104:107], 0\n\t"
    "v_mul_f32 v19, v19, v129\n\t"
    "v_mul_f32 v20, v20, v129\n\t"
    "v_mul_f32 v21, v21, v129\n\t"
    "v_mfma_f32_16x16x32_bf16 v[24:27], a[152:155], v[112:115], v[24:27]\n\t"
    "v_mul_f32 v22, v22, v129\n\t"
    "v_mul_f32 v23, v23, v129\n\t"
    "v_cvt_pk_bf16_f32 v16, v16, v17\n\t"
    "v_mfma_f32_16x16x32_bf16 v[28:31], a[164:167], v[112:115], v[28:31]\n\t"
    "v_cvt_pk_bf16_f32 v17, v18, v19\n\t"
    "v_cvt_pk_bf16_f32 v18, v20, v21\n\t"
    "v_cvt_pk_bf16_f32 v19, v22, v23\n\t"
    "v_mfma_f32_16x16x32_bf16 v[24:27], a[156:159], v[120:123], v[24:27]\n\t"
    "global_store_dwordx4 v3, v[16:19], s[50:51] offset:64 sc1\n\t"
    "v_mfma_f32_16x16x32_bf16 v[28:31], a[168:171], v[120:123], v[28:31]\n\t"
    "v_mfma_f32_16x16x32_bf16 v[212:215], a[100:103], v[132:135], 0\n\t"
    "s_nop 4\n\t"
    "v_mul_f32 v24, v24, v129\n\t"
    "v_mul_f32 v25, v25, v129\n\t"
    "v_mul_f32 v26, v26, v129\n\t"
    "v_mfma_f32_16x16x32_bf16 v[216:219], a[112:115], v[132:135], 0\n\t"
    "v_mul_f32 v27, v27, v129\n\t"
    "v_mul_f32 v28, v28, v129\n\t"
    "v_mul_f32 v29, v29, v129\n\t"
    "v_mfma_f32_16x16x32_bf16 v[212:215], a[104:107], v[140:143], v[212:215]\n\t"
    "v_mul_f32 v30, v30, v129\n\t"
    "v_mul_f32 v31, v31, v129\n\t"
    "v_cvt_pk_bf16_f32 v24, v24, v25\n\t"
    "v_mfma_f32_16x16x32_bf16 v[216:219], a[116:119], v[140:143], v[216:219]\n\t"
    "v_cvt_pk_bf16_f32 v25, v26, v27\n\t"
    "v_cvt_pk_bf16_f32 v26, v28, v29\n\t"
    "v_cvt_pk_bf16_f32 v27, v30, v31\n\t"
    "v_mfma_f32_16x16x32_bf16 v[212:215], a[108:111], v[148:151], v[212:215]\n\t"
    "global_store_dwordx4 v3, v[24:27], s[50:51] offset:128 sc1\n\t"
    "v_add_u32 v3, 86016, v3\n\t"
    "v_mfma_f32_16x16x32_bf16 v[216:219], a[120:123], v[148:151], v[216:219]\n\t"
    "v_mfma_f32_16x16x32_bf16 v[220:223], a[124:127], v[132:135], 0\n\t"
    "s_nop 3\n\t"
    "v_mul_f32 v212, v212, v157\n\t"
    "v_mul_f32 v213, v213, v157\n\t"
    "v_mul_f32 v214, v214, v157\n\t"
    "v_mfma_f32_16x16x32_bf16 v[224:227], a[136:139], v[132:135], 0\n\t"
    "v_mul_f32 v215, v215, v157\n\t"
    "v_mul_f32 v216, v216, v157\n\t"
    "v_mul_f32 v217, v217, v157\n\t"
    "v_mfma_f32_16x16x32_bf16 v[220:223], a[128:131], v[140:143], v[220:223]\n\t"
    "v_mul_f32 v218, v218, v157\n\t"
    "v_mul_f32 v219, v219, v157\n\t"
    "v_cvt_pk_bf16_f32 v212, v212, v213\n\t"
    "v_mfma_f32_16x16x32_bf16 v[224:227], a[140:143], v[140:143], v[224:227]\n\t"
    "v_cvt_pk_bf16_f32 v213, v214, v215\n\t"
    "v_cvt_pk_bf16_f32 v214, v216, v217\n\t"
    "v_cvt_pk_bf16_f32 v215, v218, v219\n\t"
    "v_mfma_f32_16x16x32_bf16 v[220:223], a[132:135], v[148:151], v[220:223]\n\t"
    "global_store_dwordx4 v3, v[212:215], s[50:51] offset:0 sc1\n\t"
    "v_mfma_f32_16x16x32_bf16 v[224:227], a[144:147], v[148:151], v[224:227]\n\t"
    "v_mfma_f32_16x16x32_bf16 v[228:231], a[148:151], v[132:135], 0\n\t"
    "s_nop 4\n\t"
    "v_mul_f32 v220, v220, v157\n\t"
    "v_mul_f32 v221, v221, v157\n\t"
    "v_mul_f32 v222, v222, v157\n\t"
    "v_mfma_f32_16x16x32_bf16 v[232:235], a[160:163], v[132:135], 0\n\t"
    "v_mul_f32 v223, v223, v157\n\t"
    "v_mul_f32 v224, v224, v157\n\t"
    "v_mul_f32 v225, v225, v157\n\t"
    "v_mfma_f32_16x16x32_bf16 v[228:231], a[152:155], v[140:143], v[228:231]\n\t"
    "v_mul_f32 v226, v226, v157\n\t"
    "v_mul_f32 v227, v227, v157\n\t"
    "v_cvt_pk_bf16_f32 v220, v220, v221\n\t"
    "v_mfma_f32_16x16x32_bf16 v[232:235], a[164:167], v[140:143], v[232:235]\n\t"
    "v_cvt_pk_bf16_f32 v221, v222, v223\n\t"
    "v_cvt_pk_bf16_f32 v222, v224, v225\n\t"
    "v_cvt_pk_bf16_f32 v223, v226, v227\n\t"
    "v_mfma_f32_16x16x32_bf16 v[228:231], a[156:159], v[148:151], v[228:231]\n\t"
    "global_store_dwordx4 v3, v[220:223], s[50:51] offset:64 sc1\n\t"
    "v_mfma_f32_16x16x32_bf16 v[232:235], a[168:171], v[148:151], v[232:235]\n\t"
    "s_nop 5\n\t"
    "v_mul_f32 v228, v228, v157\n\t"
    "v_mul_f32 v229, v229, v157\n\t"
    "v_mul_f32 v230, v230, v157\n\t"
    "v_mul_f32 v231, v231, v157\n\t"
    "v_mul_f32 v232, v232, v157\n\t"
    "v_mul_f32 v233, v233, v157\n\t"
    "v_mul_f32 v234, v234, v157\n\t"
    "v_mul_f32 v235, v235, v157\n\t"
    "v_cvt_pk_bf16_f32 v228, v228, v229\n\t"
    "v_cvt_pk_bf16_f32 v229, v230, v231\n\t"
    "v_cvt_pk_bf16_f32 v230, v232, v233\n\t"
    "v_cvt_pk_bf16_f32 v231, v234, v235\n\t"
    "global_store_dwordx4 v3, v[228:231], s[50:51] offset:128 sc1\n\t"
    "s_mov_b32 s44, 0\n\t"
    "v_mov_b32 v0, 0x5d800000\n\t"
    "v_cmp_nlt_f32 vcc, v72, v0\n\t"
    "s_nop 0\n\t"
    "s_cmp_lg_u64 vcc, 0\n\t"
    "s_cselect_b32 s23, 1, 0\n\t"
    "s_or_b32 s44, s44, s23\n\t"
    "v_cmp_nlt_f32 vcc, v100, v0\n\t"
    "s_nop 0\n\t"
    "s_cmp_lg_u64 vcc, 0\n\t"
    "s_cselect_b32 s23, 2, 0\n\t"
    "s_or_b32 s44, s44, s23\n\t"
    "v_cmp_nlt_f32 vcc, v128, v0\n\t"
    "s_nop 0\n\t"
    "s_cmp_lg_u64 vcc, 0\n\t"
    "s_cselect_b32 s23, 4, 0\n\t"
    "s_or_b32 s44, s44, s23\n\t"
    "v_cmp_nlt_f32 vcc, v156, v0\n\t"
    "s_nop 0\n\t"
    "s_cmp_lg_u64 vcc, 0\n\t"
    "s_cselect_b32 s23, 8, 0\n\t"
    "s_or_b32 s44, s44, s23\n\t"
    "s_cbranch_scc0 9f\n\t"
    "v_add_u32 v0, s39, v7\n\t"
    "global_store_dwordx4 v0, v[160:163], s[16:17] offset:0\n\t"
    "global_store_dwordx4 v0, v[164:167], s[16:17] offset:1024\n\t"
    "global_store_dwordx4 v0, v[168:171], s[16:17] offset:2048\n\t"
    "global_store_dwordx4 v0, v[172:175], s[16:17] offset:3072\n\t"
    "v_add_u32 v0, 4096, v0\n\t"
    "global_store_dwordx4 v0, v[176:179], s[16:17] offset:0\n\t"
    "global_store_dwordx4 v0, v[180:183], s[16:17] offset:1024\n\t"
    "global_store_dwordx4 v0, v[184:187], s[16:17] offset:2048\n\t"
    "global_store_dwordx4 v0, v[188:191], s[16:17] offset:3072\n\t"
    "v_add_u32 v0, 4096, v0\n\t"
    "global_store_dwordx4 v0, v[192:195], s[16:17] offset:0\n\t"
    "global_store_dwordx4 v0, v[196:199], s[16:17] offset:1024\n\t"
    "global_store_dwordx4 v0, v[200:203], s[16:17] offset:2048\n\t"
    "global_store_dwordx4 v0, v[204:207], s[16:17] offset:3072\n\t"
    "v_add_u32 v0, 4096, v0\n\t"
    "global_store_dwordx4 v0, a[100:103], s[16:17] offset:0\n\t"
    "global_store_dwordx4 v0, a[104:107], s[16:17] offset:1024\n\t"
    "global_store_dwordx4 v0, a[108:111], s[16:17] offset:2048\n\t"
    "global_store_dwordx4 v0, a[112:115], s[16:17] offset:3072\n\t"
    "v_add_u32 v0, 4096, v0\n\t"
    "global_store_dwordx4 v0, a[116:119], s[16:17] offset:0\n\t"
    "global_store_dwordx4 v0, a[120:123], s[16:17] offset:1024\n\t"
    "global_store_dwordx4 v0, a[124:127], s[16:17] offset:2048\n\t"
    "global_store_dwordx4 v0, a[128:131], s[16:17] offset:3072\n\t"
    "v_add_u32 v0, 4096, v0\n\t"
    "global_store_dwordx4 v0, a[132:135], s[16:17] offset:0\n\t"
    "global_store_dwordx4 v0, a[136:139], s[16:17] offset:1024\n\t"
    "global_store_dwordx4 v0, a[140:143], s[16:17] offset:2048\n\t"
    "global_store_dwordx4 v0, a[144:147], s[16:17] offset:3072\n\t"
    "v_add_u32 v0, 4096, v0\n\t"
    "global_store_dwordx4 v0, a[148:151], s[16:17] offset:0\n\t"
    "global_store_dwordx4 v0, a[152:155], s[16:17] offset:1024\n\t"
    "global_store_dwordx4 v0, a[156:159], s[16:17] offset:2048\n\t"
    "global_store_dwordx4 v0, a[160:163], s[16:17] offset:3072\n\t"
    "v_add_u32 v0, 4096, v0\n\t"
    "global_store_dwordx4 v0, a[164:167], s[16:17] offset:0\n\t"
    "global_store_dwordx4 v0, a[168:171], s[16:17] offset:1024\n\t"
    "s_waitcnt vmcnt(0)\n\t"
    "9:\n\t"
    "s_nop 1\n\t"

    : "+{v0}"(la), "+{v1}"(lb), "+{v3}"(oa), "+{s20}"(s20), "+{s22}"(s22), "+{v[8:11]}"(sel0), "+{v[12:15]}"(sel1), "={s45}"(epoch),
      "={s44}"(bad),
      "+{v211}"(xw0), "+{v212}"(xw1), "+{v213}"(xt0), "+{v214}"(xt1), "+{v215}"(xr), "+{v216}"(pr), "+{v217}"(pk), "+{v218}"(pv), "+{v219}"(fa), "+{v220}"(wb)
    : "{s[28:29]}"(wq), "{v208}"(vq), "{s[30:31]}"(wkv), "{s34}"((wid - 1) * 6144), "{v209}"(vkv), "{v2}"(lane * 8), "{v4}"(ga), "{v6}"(ones), "{v7}"(lane16), "{s[16:19]}"(srd), "{s25}"(woff), "{s36}"(wid), "{s37}"(FLAGS + tile * FSTR + (blk & 7) * 4), "{s38}"(zf), "{s39}"(park), "{s[50:51]}"(out)
    : "v16", "v17", "v18", "v19", "v20", "v21", "v22", "v23", "v24", "v25", "v26", "v27", "v28", "v29", "v30", "v31", "v32", "v33", "v34", "v35", "v36", "v37", "v38", "v39", "v40", "v41", "v42", "v43", "v44", "v45", "v46", "v47", "v48", "v49", "v50", "v51", "v52", "v53", "v54", "v55", "v56", "v57", "v58", "v59", "v60", "v61", "v62", "v63", "v64", "v65", "v66", "v67", "v68", "v69", "v70", "v71", "v72", "v73", "v74", "v75", "v76", "v77", "v78", "v79", "v80", "v81", "v82", "v83", "v84", "v85", "v86", "v87", "v88", "v89", "v90", "v91", "v92", "v93", "v94", "v95", "v96", "v97", "v98", "v99", "v100", "v101", "v102", "v103", "v104", "v105", "v106", "v107", "v108", "v109", "v110", "v111", "v112", "v113", "v114", "v115", "v116", "v117", "v118", "v119", "v120", "v121", "v122", "v123", "v124", "v125", "v126", "v127", "v128", "v129", "v130", "v131", "v132", "v133", "v134", "v135", "v136", "v137", "v138", "v139", "v140", "v141", "v142", "v143", "v144", "v145", "v146", "v147", "v148", "v149", "v150", "v151", "v152", "v153", "v154", "v155", "v156", "v157", "v158", "v159", "v160", "v161", "v162", "v163", "v164", "v165", "v166", "v167", "v168", "v169", "v170", "v171", "v172", "v173", "v174", "v175", "v176", "v177", "v178", "v179", "v180", "v181", "v182", "v183", "v184", "v185", "v186", "v187", "v188", "v189", "v190", "v191", "v192", "v193", "v194", "v195", "v196", "v197", "v198", "v199", "v200", "v201", "v202", "v203", "v204", "v205", "v206", "v207", "v210", "v221", "v222", "v223", "v224", "v225", "v226", "v227", "v228", "v229", "v230", "v231", "v232", "v233", "v234", "v235", "a0", "a1", "a2", "a3", "a4", "a5", "a6", "a7", "a8", "a9", "a10", "a11", "a12", "a13", "a14", "a15", "a16", "a17", "a18", "a19", "a20", "a21", "a22", "a23", "a24", "a25", "a26", "a27", "a28", "a29", "a30", "a31", "a32", "a33", "a34", "a35", "a36", "a37", "a38", "a39", "a40", "a41", "a42", "a43", "a44", "a45", "a46", "a47", "a48", "a49", "a50", "a51", "a52", "a53", "a54", "a55", "a56", "a57", "a58", "a59", "a60", "a61", "a62", "a63", "a64", "a65", "a66", "a67", "a68", "a69", "a70", "a71", "a72", "a73", "a74", "a75", "a76", "a77", "a78", "a79", "a80", "a81", "a82", "a83", "a84", "a85", "a86", "a87", "a88", "a89", "a90", "a91", "a92", "a93", "a94", "a95", "a96", "a97", "a98", "a99", "a100", "a101", "a102", "a103", "a104", "a105", "a106", "a107", "a108", "a109", "a110", "a111", "a112", "a113", "a114", "a115", "a116", "a117", "a118", "a119", "a120", "a121", "a122", "a123", "a124", "a125", "a126", "a127", "a128", "a129", "a130", "a131", "a132", "a133", "a134", "a135", "a136", "a137", "a138", "a139", "a140", "a141", "a142", "a143", "a144", "a145", "a146", "a147", "a148", "a149", "a150", "a151", "a152", "a153", "a154", "a155", "a156", "a157", "a158", "a159", "a160", "a161", "a162", "a163", "a164", "a165", "a166", "a167", "a168", "a169", "a170", "a171", "a172", "a173", "a174", "a175", "a176", "a177", "a178", "a179", "a180", "a181", "a182", "a183", "a184", "a185", "a186", "a187", "a188", "a189", "a190", "a191", "a192", "a193", "a194", "a195", "a196", "a197", "a198", "a199", "a200", "a201", "a202", "a203", "a204", "a205", "a206", "a207", "a208", "a209", "a210", "a211", "a212", "a213", "a214", "a215", "a216", "a217", "a218", "a219", "a220", "a221", "a222", "a223", "a224", "a225", "a226", "a227", "a228", "a229", "a230", "a231", "a232", "a233", "a234", "a235", "a236", "a237", "a238", "a239", "a240", "a241", "a242", "a243", "s23", "s24", "vcc", "scc", "memory");

    if (blk == 0 && tid == 0) __hip_atomic_store((unsigned*)(ws + EPOCH), epoch, __ATOMIC_RELAXED, __HIP_MEMORY_SCOPE_AGENT);
    if (__builtin_expect(bad != 0, 0)) {
        __builtin_amdgcn_fence(__ATOMIC_ACQUIRE, "agent");
#pragma unroll 1
        for (int q = 0; q < 4; ++q)
            if (bad >> q & 1) slow_tile(ws + park + lane * 16, q, ws, out + qa + q * (2 * IMG * 192), lane);
    }

}

extern "C" void kernel_launch(void* const* d_in, const int* in_sizes, int n_in, void* d_out, int out_size, void* d_ws, size_t ws_size, hipStream_t stream) {
    (void)hipFuncSetAttribute((const void*)attn_kernel, hipFuncAttributeMaxDynamicSharedMemorySize, LDSB);
    attn_kernel<<<NBLK_MAIN + 1, 256, LDSB, stream>>>((const char*)d_in[0], (const char*)d_in[1], (const char*)d_in[2], (const char*)d_in[3], (char*)d_ws, (char*)d_out);
}
```
